```python
import torch

def _softmax(x, axis=-1):
    return torch.softmax(x, dim=axis)
def _gelu(x):
    return torch.nn.functional.gelu(x, approximate="tanh")
def _silu(x):
    return torch.nn.functional.silu(x)
def _sigmoid(x):
    return torch.sigmoid(x)
def _layernorm(x, axis=-1, eps=1e-5):
    m = x.mean(dim=axis, keepdim=True)
    v = x.var(dim=axis, keepdim=True, unbiased=False)
    return (x - m) / torch.sqrt(v + eps)
def _scatter_add(tgt, idx, src, axis=0):
    return tgt.clone().index_add_(axis, idx, src)
def _segment_sum(src, seg, n):
    out = torch.zeros((n,) + src.shape[1:], dtype=src.dtype)
    return out.index_add_(0, seg, src)
def _pad(x, pw):
    flat = tuple(v for pair in reversed(pw) for v in pair)
    return torch.nn.functional.pad(x, flat)

kernel_name = 'gated_mix__gqa_block_cached__dense_activation_gelu__decode'

def setup_inputs():

    _w = (lambda a: (lambda t: t.to(torch.bfloat16) if t.is_floating_point() else t)(torch.as_tensor(a)))
    def _A_setup():
        B, Sq, D = 1, 1, 448
        Hq, Hkv, Sk, Dh = 16, 8, 65536, 256
        assert Hq % Hkv == 0, (Hq, Hkv)
        return {
            "x": _w((lambda *s: torch.randn(*s, dtype=torch.bfloat16) * 0.05)(B, Sq, D)),
            "k_cache": _w((lambda *s: torch.randn(*s, dtype=torch.bfloat16) * 6.4)(B, Hkv, Sk, Dh)),
            "v_cache": _w((lambda *s: torch.randn(*s, dtype=torch.bfloat16) * 0.4)(B, Hkv, Sk, Dh)),
            "wq": _w((lambda *s: torch.randn(*s, dtype=torch.bfloat16) * 6.4)(D, Hq * Dh) * (D ** -0.5)),
            "wo": _w((lambda *s: torch.randn(*s, dtype=torch.bfloat16) * 0.4)(Hq * Dh, D) * ((Hq * Dh) ** -0.5)),
        }
    _A_in = _A_setup()

    def _B_setup():
        B, S, D, F = 1, 1, 448, 1792
        return {
            "x": _w((lambda *s: torch.randn(*s, dtype=torch.bfloat16) * 0.05)(B, S, D)),
            "w1": _w((lambda *s: torch.randn(*s, dtype=torch.bfloat16) * 3.2)(D, F) * (D ** -0.5)),
            "b1": _w((lambda *s: torch.randn(*s, dtype=torch.bfloat16) * 3.2)(F) * 0.01),
            "w2": _w((lambda *s: torch.randn(*s, dtype=torch.bfloat16) * 0.2)(F, D) * (F ** -0.5)),
            "b2": _w((lambda *s: torch.randn(*s, dtype=torch.bfloat16) * 0.4)(D) * 0.01),
        }
    _B_in = _B_setup()

    return {
        "A_x": _A_in["x"],
        "A_k_cache": _A_in["k_cache"],
        "A_v_cache": _A_in["v_cache"],
        "A_wq": _A_in["wq"],
        "A_wo": _A_in["wo"],
        "B_x": _B_in["x"],
        "B_w1": _B_in["w1"],
        "B_b1": _B_in["b1"],
        "B_w2": _B_in["w2"],
        "B_b2": _B_in["b2"],
        "gate_w": _w((lambda *s: torch.randn(*s, dtype=torch.bfloat16) * 0.05)(448) * 16.0),
    }

def reference(A_x, A_k_cache, A_v_cache, A_wq, A_wo, B_x, B_w1, B_b1, B_w2, B_b2, gate_w):
    def _A_ref(x, k_cache, v_cache, wq, wo):
        B, Sq, D = x.shape
        Hq, Dh = 16, 256
        Hkv = k_cache.shape[1]
        rep = Hq // Hkv
        q = (lambda x, *d: x.permute(*d))((x @ wq).reshape(B, Sq, Hq, Dh), 0, 2, 1, 3)
        k = (lambda x, n, axis: torch.repeat_interleave(x, n, dim=axis))(k_cache, rep, axis=1)
        v = (lambda x, n, axis: torch.repeat_interleave(x, n, dim=axis))(v_cache, rep, axis=1)
        scores = torch.einsum("bhqd,bhkd->bhqk", q, k) * (Dh ** -0.5)
        p = _softmax(scores, axis=-1)
        o = torch.einsum("bhqk,bhkd->bhqd", p, v)
        return (lambda x, *d: x.permute(*d))(o, 0, 2, 1, 3).reshape(B, Sq, Hq * Dh) @ wo

    def _B_ref(x, w1, b1, w2, b2):
        h = torch.einsum("bsd,df->bsf", x, w1) + b1
        h = _gelu(h)
        return torch.einsum("bsf,fd->bsd", h, w2) + b2

    x = A_x
    g = _sigmoid((x * gate_w).sum(-1, keepdims=True))
    a = _A_ref(x, A_k_cache, A_v_cache, A_wq, A_wo)
    b = _B_ref(x, B_w1, B_b1, B_w2, B_b2)
    return g * a + (1 - g) * b
```

```cpp
#include <hip/hip_runtime.h>
#include <hip/hip_bf16.h>
#include <cstdint>

constexpr int DM = 448, HQ = 16, HKV = 8, SK = 65536, DH = 256, FF = 1792;
#define NSPLIT 32
#define NWAVES 8
#define KV_NT 1
constexpr int NTHR = NWAVES * 64;
constexpr int KEYS_PER_WG = SK / NSPLIT;
constexpr int TILE = 16;
constexpr int TILES_PER_WAVE = KEYS_PER_WG / TILE / NWAVES;

using bf16x2 = __attribute__((ext_vector_type(2))) __bf16;
using u32x4  = __attribute__((ext_vector_type(4))) unsigned;
using f32x2  = __attribute__((ext_vector_type(2))) float;
using f32x4  = __attribute__((ext_vector_type(4))) float;

__device__ __forceinline__ float dot2(unsigned k, unsigned q, float acc) {
  return __builtin_amdgcn_fdot2_f32_bf16(__builtin_bit_cast(bf16x2, k), __builtin_bit_cast(bf16x2, q), acc, false);
}
template <int CTRL> __device__ __forceinline__ float dpp(float x) {
  return __builtin_bit_cast(float, __builtin_amdgcn_mov_dpp(__builtin_bit_cast(int, x), CTRL, 0xf, 0xf, true));
}
constexpr int XOR1 = 0xB1, XOR2 = 0x4E, XOR7 = 0x141, XOR8 = 0x128, ROR4 = 0x124;
__device__ __forceinline__ float bflo(unsigned u) { return __builtin_bit_cast(float, u << 16); }
__device__ __forceinline__ float bfhi(unsigned u) { return __builtin_bit_cast(float, u & 0xffff0000u); }
__device__ __forceinline__ float bf2f(unsigned short u) { return __builtin_bit_cast(float, (unsigned)u << 16); }
__device__ __forceinline__ unsigned short f2bf(float f) { __hip_bfloat16 b = __float2bfloat16(f); return __builtin_bit_cast(unsigned short, b); }
__device__ __forceinline__ u32x4 ldkv(const u32x4* p) { return KV_NT ? __builtin_nontemporal_load(p) : *p; }

constexpr size_t WS_Q = 0;
constexpr size_t WS_H = 16384;
constexpr size_t WS_O = 32768;
constexpr size_t WS_ML = 65536;
constexpr size_t WS_PO = 131072;

__global__ void __launch_bounds__(256) k_prep(const unsigned short* x, const unsigned short* wq, const unsigned short* w1, const unsigned short* b1, unsigned char* ws) {
  __shared__ float red[4][16];
  const int tid = threadIdx.x, lane = tid & 63, wave = tid >> 6;
  const bool isq = blockIdx.x < 256;
  const int ld = isq ? HQ * DH : FF;
  const unsigned short* W = isq ? wq : w1;
  const int col0 = (isq ? blockIdx.x : blockIdx.x - 256) * 16 + (tid & 1) * 8;
  float acc[8];
#pragma unroll
  for (int k = 0; k < 8; ++k) acc[k] = 0.f;
#pragma unroll
  for (int i = 0; i < 4; ++i) {
    const int row = (tid >> 1) + 128 * i;
    if (row < DM) {
      const u32x4 w = *reinterpret_cast<const u32x4*>(W + (size_t)row * ld + col0);
      const float xv = bf2f(x[row]);
#pragma unroll
      for (int k = 0; k < 4; ++k) { acc[2 * k] = fmaf(xv, bflo(w[k]), acc[2 * k]); acc[2 * k + 1] = fmaf(xv, bfhi(w[k]), acc[2 * k + 1]); }
    }
  }
#pragma unroll
  for (int k = 0; k < 8; ++k) {
#pragma unroll
    for (int o = 2; o < 64; o <<= 1) acc[k] += __shfl_xor(acc[k], o);
  }
  if (lane < 2) {
#pragma unroll
    for (int k = 0; k < 8; ++k) red[wave][lane * 8 + k] = acc[k];
  }
  __syncthreads();
  if (tid < 16) {
    float s = red[0][tid] + red[1][tid] + red[2][tid] + red[3][tid];
    if (isq) reinterpret_cast<unsigned short*>(ws + WS_Q)[blockIdx.x * 16 + tid] = f2bf(s);
    else {
      const int c = (blockIdx.x - 256) * 16 + tid;
      float h = s + bf2f(b1[c]);
      const float u = 0.7978845608028654f * (h + 0.044715f * h * h * h);
      const float g = 0.5f * h * (1.f + tanhf(u));
      reinterpret_cast<float*>(ws + WS_H)[c] = g;
    }
  }
}

__global__ void __launch_bounds__(NTHR) k_attn(const u32x4* __restrict__ Kc, const u32x4* __restrict__ Vc, unsigned char* ws) {
  __shared__ __attribute__((aligned(16))) float lds[NWAVES * 2 * (DH + 4)];
  const int g = blockIdx.x & 7, j = blockIdx.x >> 3;
  const int tid = threadIdx.x, lane = tid & 63, wave = __builtin_amdgcn_readfirstlane(tid >> 6);
  const int c = lane & 31, t = lane & 15;
  constexpr float C2 = 0.0625f * 1.4426950408889634f;
  u32x4 q0 = *reinterpret_cast<const u32x4*>(ws + WS_Q + ((size_t)(2 * g) * DH + 8 * c) * 2);
  u32x4 q1 = *reinterpret_cast<const u32x4*>(ws + WS_Q + ((size_t)(2 * g + 1) * DH + 8 * c) * 2);
  const size_t base = ((size_t)g * SK + (size_t)j * KEYS_PER_WG + (size_t)wave * TILE) * 32 + lane;
  const u32x4* kp = Kc + base;
  const u32x4* vp = Vc + base;
  constexpr size_t TSTR = (size_t)NWAVES * TILE * 32;
  u32x4 Kb[8], Vb[8];
#pragma unroll
  for (int i = 0; i < 8; ++i) Kb[i] = ldkv(kp + i * 64);
#pragma unroll
  for (int i = 0; i < 8; ++i) Vb[i] = ldkv(vp + i * 64);
  float m = -INFINITY, l = 0.f;
  f32x2 acc0[4], acc1[4];
#pragma unroll
  for (int k = 0; k < 4; ++k) { acc0[k] = f32x2{0.f, 0.f}; acc1[k] = f32x2{0.f, 0.f}; }
  for (int it = 0; it < TILES_PER_WAVE; ++it) {
    float x[16];
#pragma unroll
    for (int i = 0; i < 8; ++i) {
      float a = 0.f, b = 0.f;
#pragma unroll
      for (int k = 0; k < 4; ++k) { a = dot2(Kb[i][k], q0[k], a); b = dot2(Kb[i][k], q1[k], b); }
      x[2 * i] = a; x[2 * i + 1] = b;
    }
    const bool more = it + 1 < TILES_PER_WAVE;
    kp += TSTR;
    if (more) {
#pragma unroll
      for (int i = 0; i < 8; ++i) Kb[i] = ldkv(kp + i * 64);
    }
#define TR_STEP(HALF, CTRL, BIT) _Pragma("unroll") for (int h = 0; h < (HALF); ++h) { \
      const float lo_ = x[h], hi_ = x[h + (HALF)]; const float t1_ = lo_ + dpp<CTRL>(lo_), t2_ = hi_ + dpp<CTRL>(hi_); \
      x[h] = (t & (BIT)) ? t2_ : t1_; }
    TR_STEP(8, XOR8, 8) TR_STEP(4, XOR7, 4) TR_STEP(2, XOR2, 2) TR_STEP(1, XOR1, 1)
#undef TR_STEP
    {
      auto s = __builtin_amdgcn_permlane16_swap(__float_as_uint(x[0]), __float_as_uint(x[0]), false, false);
      x[0] = __uint_as_float(s[0]) + __uint_as_float(s[1]);
    }
    const float sc = x[0] * C2;
    if (__any(sc > m)) {
      float pm = sc;
      pm = fmaxf(pm, dpp<XOR2>(pm)); pm = fmaxf(pm, dpp<ROR4>(pm)); pm = fmaxf(pm, dpp<XOR8>(pm));
      auto s = __builtin_amdgcn_permlane32_swap(__float_as_uint(pm), __float_as_uint(pm), false, false);
      pm = fmaxf(__uint_as_float(s[0]), __uint_as_float(s[1]));
      const float mn = fmaxf(m, pm);
      const float alpha = __builtin_amdgcn_exp2f(m - mn);
      m = mn; l *= alpha;
      const float a0 = dpp<0x00>(alpha), a1 = dpp<0x55>(alpha);
#pragma unroll
      for (int k = 0; k < 4; ++k) { acc0[k] *= a0; acc1[k] *= a1; }
    }
    const float p = __builtin_amdgcn_exp2f(sc - m);
    l += p;
#define PV(i) { const float p0 = dpp<0x150 + 2 * (i)>(p), p1 = dpp<0x150 + 2 * (i) + 1>(p); \
      _Pragma("unroll") for (int k = 0; k < 4; ++k) { const f32x2 vf = {bflo(Vb[i][k]), bfhi(Vb[i][k])}; \
        acc0[k] = __builtin_elementwise_fma(f32x2{p0, p0}, vf, acc0[k]); acc1[k] = __builtin_elementwise_fma(f32x2{p1, p1}, vf, acc1[k]); } }
    PV(0) PV(1) PV(2) PV(3) PV(4) PV(5) PV(6) PV(7)
#undef PV
    vp += TSTR;
    if (more) {
#pragma unroll
      for (int i = 0; i < 8; ++i) Vb[i] = ldkv(vp + i * 64);
    }
  }
#pragma unroll
  for (int k = 0; k < 4; ++k) {
#pragma unroll
    for (int e = 0; e < 2; ++e) {
      auto s = __builtin_amdgcn_permlane32_swap(__float_as_uint(acc0[k][e]), __float_as_uint(acc0[k][e]), false, false);
      acc0[k][e] = __uint_as_float(s[0]) + __uint_as_float(s[1]);
      auto s2 = __builtin_amdgcn_permlane32_swap(__float_as_uint(acc1[k][e]), __float_as_uint(acc1[k][e]), false, false);
      acc1[k][e] = __uint_as_float(s2[0]) + __uint_as_float(s2[1]);
    }
  }
  {
    l += dpp<XOR2>(l); l += dpp<ROR4>(l); l += dpp<XOR8>(l);
    auto s = __builtin_amdgcn_permlane32_swap(__float_as_uint(l), __float_as_uint(l), false, false);
    l = __uint_as_float(s[0]) + __uint_as_float(s[1]);
  }
  {
    float* rec = lds + (wave * 2 + (lane >> 5)) * (DH + 4);
    const f32x2* a = (lane >> 5) ? acc1 : acc0;
    f32x4 v0 = {(lane >> 5) ? acc1[0][0] : acc0[0][0], (lane >> 5) ? acc1[0][1] : acc0[0][1], (lane >> 5) ? acc1[1][0] : acc0[1][0], (lane >> 5) ? acc1[1][1] : acc0[1][1]};
    f32x4 v1 = {(lane >> 5) ? acc1[2][0] : acc0[2][0], (lane >> 5) ? acc1[2][1] : acc0[2][1], (lane >> 5) ? acc1[3][0] : acc0[3][0], (lane >> 5) ? acc1[3][1] : acc0[3][1]};
    (void)a;
    *reinterpret_cast<f32x4*>(rec + 4 + 8 * c) = v0;
    *reinterpret_cast<f32x4*>(rec + 4 + 8 * c + 4) = v1;
    if (lane < 2) { float* r2 = lds + (wave * 2 + lane) * (DH + 4); r2[0] = m; r2[1] = l; }
  }
  __syncthreads();
  for (int e = tid; e < 2 * DH; e += NTHR) {
    const int h = e >> 8, d = e & 255;
    float M = -INFINITY;
#pragma unroll
    for (int w = 0; w < NWAVES; ++w) M = fmaxf(M, lds[(w * 2 + h) * (DH + 4)]);
    float L = 0.f, o = 0.f;
#pragma unroll
    for (int w = 0; w < NWAVES; ++w) {
      const float* rec = lds + (w * 2 + h) * (DH + 4);
      const float wt = __builtin_amdgcn_exp2f(rec[0] - M);
      L = fmaf(wt, rec[1], L); o = fmaf(wt, rec[4 + d], o);
    }
    const size_t slot = (size_t)(2 * g + h) * NSPLIT + j;
    reinterpret_cast<float*>(ws + WS_PO)[slot * DH + d] = o;
    if (d == 0) { float* ml = reinterpret_cast<float*>(ws + WS_ML) + slot * 2; ml[0] = M; ml[1] = L; }
  }
}

__global__ void __launch_bounds__(256) k_comb(unsigned char* ws) {
  const int h = blockIdx.x, d = threadIdx.x;
  const float* ml = reinterpret_cast<const float*>(ws + WS_ML) + (size_t)h * NSPLIT * 2;
  const float* po = reinterpret_cast<const float*>(ws + WS_PO) + (size_t)h * NSPLIT * DH;
  float M = -INFINITY;
  for (int s = 0; s < NSPLIT; ++s) M = fmaxf(M, ml[2 * s]);
  float L = 0.f, o = 0.f;
#pragma unroll 8
  for (int s = 0; s < NSPLIT; ++s) { const float w = __builtin_amdgcn_exp2f(ml[2 * s] - M); L = fmaf(w, ml[2 * s + 1], L); o = fmaf(w, po[s * DH + d], o); }
  reinterpret_cast<float*>(ws + WS_O)[h * DH + d] = o / L;
}

__global__ void __launch_bounds__(256) k_out(const unsigned short* x, const unsigned short* gw, const unsigned short* wo, const unsigned short* w2, const unsigned short* b2,
                                             const unsigned char* ws, unsigned short* out) {
  __shared__ float red[4][16];
  __shared__ float gred[4];
  const int tid = threadIdx.x, lane = tid & 63, wave = tid >> 6, col0 = blockIdx.x * 8;
  const float* o = reinterpret_cast<const float*>(ws + WS_O);
  const float* hh = reinterpret_cast<const float*>(ws + WS_H);
  float a[8], b[8];
#pragma unroll
  for (int k = 0; k < 8; ++k) { a[k] = 0.f; b[k] = 0.f; }
#pragma unroll 4
  for (int r = tid; r < HQ * DH; r += 256) {
    const u32x4 w = *reinterpret_cast<const u32x4*>(wo + (size_t)r * DM + col0);
    const float xv = o[r];
#pragma unroll
    for (int k = 0; k < 4; ++k) { a[2 * k] = fmaf(xv, bflo(w[k]), a[2 * k]); a[2 * k + 1] = fmaf(xv, bfhi(w[k]), a[2 * k + 1]); }
  }
#pragma unroll
  for (int r = tid; r < FF; r += 256) {
    const u32x4 w = *reinterpret_cast<const u32x4*>(w2 + (size_t)r * DM + col0);
    const float xv = hh[r];
#pragma unroll
    for (int k = 0; k < 4; ++k) { b[2 * k] = fmaf(xv, bflo(w[k]), b[2 * k]); b[2 * k + 1] = fmaf(xv, bfhi(w[k]), b[2 * k + 1]); }
  }
  float gs = 0.f;
  for (int r = tid; r < DM; r += 256) gs += bf2f(x[r]) * bf2f(gw[r]);
#pragma unroll
  for (int o2 = 1; o2 < 64; o2 <<= 1) {
    gs += __shfl_xor(gs, o2);
#pragma unroll
    for (int k = 0; k < 8; ++k) { a[k] += __shfl_xor(a[k], o2); b[k] += __shfl_xor(b[k], o2); }
  }
  if (lane == 0) {
    gred[wave] = gs;
#pragma unroll
    for (int k = 0; k < 8; ++k) { red[wave][k] = a[k]; red[wave][8 + k] = b[k]; }
  }
  __syncthreads();
  if (tid < 8) {
    const float gsum = gred[0] + gred[1] + gred[2] + gred[3];
    const float g = 1.f / (1.f + __expf(-gsum));
    const float av = red[0][tid] + red[1][tid] + red[2][tid] + red[3][tid];
    const float bv = red[0][8 + tid] + red[1][8 + tid] + red[2][8 + tid] + red[3][8 + tid] + bf2f(b2[col0 + tid]);
    out[col0 + tid] = f2bf(g * av + (1.f - g) * bv);
  }
}

extern "C" void kernel_launch(void* const* d_in, const int* in_sizes, int n_in, void* d_out, int out_size, void* d_ws, size_t ws_size, hipStream_t stream) {
  const unsigned short* x  = (const unsigned short*)d_in[0];
  const u32x4* K = (const u32x4*)d_in[1];
  const u32x4* V = (const u32x4*)d_in[2];
  const unsigned short* wq = (const unsigned short*)d_in[3];
  const unsigned short* wo = (const unsigned short*)d_in[4];
  const unsigned short* w1 = (const unsigned short*)d_in[6];
  const unsigned short* b1 = (const unsigned short*)d_in[7];
  const unsigned short* w2 = (const unsigned short*)d_in[8];
  const unsigned short* b2 = (const unsigned short*)d_in[9];
  const unsigned short* gw = (const unsigned short*)d_in[10];
  unsigned char* ws = (unsigned char*)d_ws;
  k_prep<<<256 + FF / 16, 256, 0, stream>>>(x, wq, w1, b1, ws);
  k_attn<<<HKV * NSPLIT, NTHR, 0, stream>>>(K, V, ws);
  k_comb<<<HQ, 256, 0, stream>>>(ws);
  k_out<<<DM / 8, 256, 0, stream>>>(x, gw, wo, w2, b2, ws, (unsigned short*)d_out);
}
```

```cpp
#include <hip/hip_runtime.h>
#include <hip/hip_bf16.h>
#include <cstdint>

#define NSS 3
#define NH 3
#define HD 6
#define NL 4
#define RATE 7500
#define START_T 30
#define CHK_T 3000
#define WP 0
#define WT0 900
#define PRIO 1
#define NSW (NSS + NH)
#define MODE 0
#define POOLT 0
#define CHT 4
#define W_AUX 2
#define X_NOCLEAR 0
#define X_NOW 0
#define STAMPS 0

constexpr int DM = 448, HQ = 16, HKV = 8, SK = 65536, DH = 256, FF = 1792;
constexpr int NWG = 256, NJ = 32, NTHR = (NSW + 2) * 64;
constexpr int TILE_KEYS = 2 * NL, TILE_BYTES = TILE_KEYS * DH * 2;
constexpr int RANGE_TILES = SK / TILE_KEYS / NJ;
constexpr int ST_TILES = RANGE_TILES - POOLT, POOL_CHUNKS = NJ * POOLT / CHT;
static_assert(NSW >= 4 && NH * HD + 2 * NSS <= RANGE_TILES && HD >= 1 && HD <= 6, "deal");
constexpr unsigned TAU = RATE ? (unsigned)(NL * 1024.0 * NWG / RATE * 1e-3 * 100 * 64 + 0.5) : 0;
constexpr unsigned OOB = 0x80000000u;
constexpr int NRED = 28;
constexpr unsigned SENT = 0xFFFFFFFFu, TK_INVALID = 0xFFFFFFFFu, TK_END = 0xFFFFFFFEu;
constexpr unsigned SPIN_CAP = 1u << 20;

constexpr unsigned WS_EPOCH = 0, WS_CTR = 256, WS_CL = 4096, WS_QG = 8192, WS_ML = WS_QG + 4096 * 8, WS_P1 = WS_ML + 4096,
                   WS_Y = WS_P1 + HKV * NJ * NJ * 64, WS_END = WS_Y + NRED * NWG * 64, WS_STAMP = 2u << 20;
constexpr int L_Q = 0, L_CTR = 256, L_LINE = 258, L_FIN = 260, L_Y2 = 328, L_TK = L_Y2 + 520, L_REC = (L_TK + POOL_CHUNKS + 64 + 3) & ~3, REC = 516, L_END = L_REC + NSW * REC;

using bf16x2 = __attribute__((ext_vector_type(2))) __bf16;
using u32x2  = __attribute__((ext_vector_type(2))) unsigned;
using u32x3  = __attribute__((ext_vector_type(3))) unsigned;
using u32x4  = __attribute__((ext_vector_type(4))) unsigned;
using f32x2  = __attribute__((ext_vector_type(2))) float;
using f32x4  = __attribute__((ext_vector_type(4))) float;
#define LAS __attribute__((address_space(3)))
#define GAS __attribute__((address_space(1)))
typedef GAS unsigned gu32; typedef GAS unsigned long long gu64;
#define RLX_AGENT __ATOMIC_RELAXED, __HIP_MEMORY_SCOPE_AGENT
#define RLX_WG __ATOMIC_RELAXED, __HIP_MEMORY_SCOPE_WORKGROUP
#define CBAR() asm volatile("" ::: "memory")

__device__ __forceinline__ float dot2(unsigned k, unsigned q, float acc) {
  return __builtin_amdgcn_fdot2_f32_bf16(__builtin_bit_cast(bf16x2, k), __builtin_bit_cast(bf16x2, q), acc, false);
}
template <int CTRL> __device__ __forceinline__ float dpp(float x) {
  return __builtin_bit_cast(float, __builtin_amdgcn_mov_dpp(__builtin_bit_cast(int, x), CTRL, 0xf, 0xf, true));
}
constexpr int XOR1 = 0xB1, XOR2 = 0x4E, XOR7 = 0x141, XOR8 = 0x128, ROR4 = 0x124, ROR12 = 0x12C;
__device__ __forceinline__ float bflo(unsigned u) { return __builtin_bit_cast(float, u << 16); }
__device__ __forceinline__ float bfhi(unsigned u) { return __builtin_bit_cast(float, u & 0xffff0000u); }
__device__ __forceinline__ unsigned f2bf(float f) { __hip_bfloat16 b = __float2bfloat16(f); return __builtin_bit_cast(unsigned short, b); }
__device__ __forceinline__ float sw16_add(float x) { auto s = __builtin_amdgcn_permlane16_swap(__float_as_uint(x), __float_as_uint(x), false, false); return __uint_as_float(s[0]) + __uint_as_float(s[1]); }
__device__ __forceinline__ float sw32_add(float x) { auto s = __builtin_amdgcn_permlane32_swap(__float_as_uint(x), __float_as_uint(x), false, false); return __uint_as_float(s[0]) + __uint_as_float(s[1]); }
__device__ __forceinline__ float sw16_max(float x) { auto s = __builtin_amdgcn_permlane16_swap(__float_as_uint(x), __float_as_uint(x), false, false); return fmaxf(__uint_as_float(s[0]), __uint_as_float(s[1])); }
__device__ __forceinline__ float sw32_max(float x) { auto s = __builtin_amdgcn_permlane32_swap(__float_as_uint(x), __float_as_uint(x), false, false); return fmaxf(__uint_as_float(s[0]), __uint_as_float(s[1])); }
__device__ __forceinline__ float red_q(float x) { x += dpp<ROR4>(x); x += dpp<XOR8>(x); x = sw16_add(x); return sw32_add(x); }
__device__ __forceinline__ float red_all(float x) { x += dpp<XOR1>(x); x += dpp<XOR2>(x); return red_q(x); }
__device__ __forceinline__ unsigned nosent(float f) { const unsigned u = __float_as_uint(f); return u == SENT ? 0x7FFFFFFFu : u; }
__device__ __forceinline__ unsigned long long now() { return __builtin_amdgcn_s_memrealtime(); }
#define STAMP(i) do { if (STAMPS) { if (lane == 0) reinterpret_cast<unsigned long long*>(ws + WS_STAMP)[b * 32 + (i)] = now(); } } while (0)

__global__ void __launch_bounds__(NTHR, (NSW + 5) / 4)
k_fused(const unsigned short* __restrict__ x, const unsigned char* __restrict__ Kc, const unsigned char* __restrict__ Vc,
        const unsigned short* __restrict__ wq, const unsigned short* __restrict__ wo, const unsigned short* __restrict__ w1,
        const unsigned short* __restrict__ b1, const unsigned short* __restrict__ w2, const unsigned short* __restrict__ b2,
        const unsigned short* __restrict__ gw, unsigned short* __restrict__ out, unsigned char* ws) {
  __shared__ __attribute__((aligned(16))) unsigned lds[L_END];
  const int b = blockIdx.x, g = b >> 5, j = b & 31;
  const int tid = threadIdx.x, lane = tid & 63, wave = __builtin_amdgcn_readfirstlane(tid >> 6);
  const auto rws = __builtin_amdgcn_make_buffer_rsrc((void*)ws, 0, (int)WS_END, 0x00020000);
  const int xg = b & 7, xi = b >> 3;

  if (wave >= 2) {
    const int sw = wave - 2, c = lane & 31, t = lane & 15;
    constexpr float C2 = 0.0625f * 1.4426950408889634f;
    const auto rk = __builtin_amdgcn_make_buffer_rsrc((void*)Kc, 0, HKV * SK * DH * 2, 0x00020000);
    const auto rv = __builtin_amdgcn_make_buffer_rsrc((void*)Vc, 0, HKV * SK * DH * 2, 0x00020000);
    const unsigned hbase = (unsigned)g * (unsigned)(SK * DH * 2);
    const unsigned voff = lane * 16;
    const unsigned rbase = hbase + (unsigned)(j * RANGE_TILES) * TILE_BYTES;
    const unsigned t0 = (unsigned)now();
#define SCHED(slot) do { while (((unsigned)now() - t0) * 64u < START_T * 64u + (unsigned)(slot) * TAU) __builtin_amdgcn_s_sleep(1); } while (0)
#define LDK(i, so_) __builtin_amdgcn_raw_buffer_load_b128(rk, voff + (i) * 1024, so_, 2)
#define LDV(i, so_) __builtin_amdgcn_raw_buffer_load_b128(rv, voff + (i) * 1024, so_, 2)
#define SB() __builtin_amdgcn_sched_barrier(0)
    u32x4 q0, q1;
    float m = -INFINITY, l = 0.f;
    f32x2 acc0[4], acc1[4];
#pragma unroll
    for (int k = 0; k < 4; ++k) { acc0[k] = f32x2{0.f, 0.f}; acc1[k] = f32x2{0.f, 0.f}; }
    float x_[2 * NL], p;
#define GETQ() do { q0 = *reinterpret_cast<const u32x4*>(&lds[L_Q + 4 * c]); q1 = *reinterpret_cast<const u32x4*>(&lds[L_Q + 128 + 4 * c]); } while (0)
#define QK1(KK, i) { float a_ = 0.f, b_ = 0.f; \
      _Pragma("unroll") for (int k = 0; k < 4; ++k) { a_ = dot2((KK)[k], q0[k], a_); b_ = dot2((KK)[k], q1[k], b_); } \
      asm volatile("" : "+v"(a_), "+v"(b_)); x_[2 * (i)] = a_; x_[2 * (i) + 1] = b_; }
#define TR_STEP(HALF, CTRL, BIT) _Pragma("unroll") for (int h = 0; h < (HALF); ++h) { \
      const float lo_ = x_[h], hi_ = x_[h + (HALF)]; const float t1_ = lo_ + dpp<CTRL>(lo_), t2_ = hi_ + dpp<CTRL>(hi_); \
      x_[h] = (t & (BIT)) ? t2_ : t1_; }
#define SOFTMAX() { TR_STEP(4, XOR8, 8) TR_STEP(2, XOR7, 4) TR_STEP(1, XOR2, 2) x_[0] += dpp<XOR1>(x_[0]); \
      const float sc = sw16_add(x_[0]) * C2; \
      if (__any(sc > m)) {                                       \
        float pm = sc; pm = fmaxf(pm, dpp<ROR4>(pm)); pm = fmaxf(pm, dpp<XOR8>(pm)); pm = sw32_max(pm); \
        const float mn = fmaxf(m, pm), alpha = __builtin_amdgcn_exp2f(m - mn); \
        m = mn; l *= alpha; \
        const float a0 = dpp<0x00>(alpha), a1 = dpp<0xAA>(alpha); \
        _Pragma("unroll") for (int k = 0; k < 4; ++k) { acc0[k] *= a0; acc1[k] *= a1; } } \
      p = __builtin_amdgcn_exp2f(sc - m); l += p; }
#define PV1(VV, i) { const float p0 = dpp<0x150 + 4 * (i)>(p), p1 = dpp<0x150 + 4 * (i) + 2>(p); \
      _Pragma("unroll") for (int k = 0; k < 4; ++k) { const f32x2 vf = {bflo((VV)[k]), bfhi((VV)[k])}; \
        acc0[k] = __builtin_elementwise_fma(f32x2{p0, p0}, vf, acc0[k]); acc1[k] = __builtin_elementwise_fma(f32x2{p1, p1}, vf, acc1[k]); } \
      _Pragma("unroll") for (int k = 0; k < 4; ++k) { asm volatile("" : "+v"(acc0[k])); asm volatile("" : "+v"(acc1[k])); } }
    if (sw >= NSS) {
      const int h_ = sw - NSS;
      u32x4 Kb[HD][NL], Vb[HD][NL];
#pragma unroll
      for (int d = 0; d < HD; ++d) {
        const unsigned so = rbase + (unsigned)(d * NH + h_) * TILE_BYTES;
        SCHED(2 * d * NH + h_);
#pragma unroll
        for (int i = 0; i < NL; ++i) Kb[d][i] = LDK(i, so);
        SB();
        SCHED((2 * d + 1) * NH + h_);
#pragma unroll
        for (int i = 0; i < NL; ++i) Vb[d][i] = LDV(i, so);
        SB();
      }
      __syncthreads();
      GETQ();
#pragma unroll
      for (int d = 0; d < HD; ++d) {
        QK1(Kb[d][0], 0) QK1(Kb[d][1], 1) QK1(Kb[d][2], 2) QK1(Kb[d][3], 3)
        SOFTMAX();
        PV1(Vb[d][0], 0) PV1(Vb[d][1], 1) PV1(Vb[d][2], 2) PV1(Vb[d][3], 3)
        SB();
      }
    } else {
      u32x4 Kb[NL], Vb[NL];
      unsigned sidx = NH * HD + sw;
      unsigned so = rbase + sidx * TILE_BYTES;
      if (PRIO) __builtin_amdgcn_s_setprio(PRIO);
      SCHED(2 * NH * HD + sw);
#pragma unroll
      for (int i = 0; i < NL; ++i) Kb[i] = LDK(i, so);
      SB();
      SCHED(2 * NH * HD + NSS + sw);
#pragma unroll
      for (int i = 0; i < NL; ++i) Vb[i] = LDV(i, so);
      SB();
      if (sw == 0) STAMP(24);
      __syncthreads();
      GETQ();
      do {
        sidx += NSS;
        so = sidx < (unsigned)RANGE_TILES ? rbase + sidx * TILE_BYTES : OOB;
        if (STAMPS && sw == 0 && (sidx & 31) < NSS) STAMP(16 + ((sidx >> 5) & 7));
        QK1(Kb[0], 0) SB(); Kb[0] = LDK(0, so); SB();
        QK1(Kb[1], 1) SB(); Kb[1] = LDK(1, so); SB();
        QK1(Kb[2], 2) SB(); Kb[2] = LDK(2, so); SB();
        QK1(Kb[3], 3) SB(); Kb[3] = LDK(3, so); SB();
        SOFTMAX();
        PV1(Vb[0], 0) SB(); Vb[0] = LDV(0, so); SB();
        PV1(Vb[1], 1) SB(); Vb[1] = LDV(1, so); SB();
        PV1(Vb[2], 2) SB(); Vb[2] = LDV(2, so); SB();
        PV1(Vb[3], 3) SB(); Vb[3] = LDV(3, so); SB();
      } while (so != OOB);
    }
    if (sw == 0) STAMP(3);
    float r[8];
#pragma unroll
    for (int k = 0; k < 4; ++k)
#pragma unroll
      for (int e = 0; e < 2; ++e) {
        auto s = __builtin_amdgcn_permlane32_swap(__float_as_uint(acc0[k][e]), __float_as_uint(acc1[k][e]), false, false);
        r[2 * k + e] = __uint_as_float(s[0]) + __uint_as_float(s[1]);
      }
    l += dpp<ROR4>(l); l += dpp<XOR8>(l); l = sw32_add(l);
    float* rec = reinterpret_cast<float*>(&lds[L_REC + sw * REC]);
    *reinterpret_cast<f32x4*>(rec + 4 + 8 * lane) = f32x4{r[0], r[1], r[2], r[3]};
    *reinterpret_cast<f32x4*>(rec + 8 + 8 * lane) = f32x4{r[4], r[5], r[6], r[7]};
    if (lane == 0 || lane == 2) { rec[lane ? 2 : 0] = m; rec[lane ? 3 : 1] = l; }
    __syncthreads();
    if (sw < 2) {
      const int h = sw;
      const float* rc = reinterpret_cast<const float*>(&lds[L_REC]);
      float M = -INFINITY;
#pragma unroll
      for (int w = 0; w < NSW; ++w) M = fmaxf(M, rc[w * REC + 2 * h]);
      float L = 0.f; f32x4 o = {0.f, 0.f, 0.f, 0.f};
#pragma unroll
      for (int w = 0; w < NSW; ++w) {
        const float wt = __builtin_amdgcn_exp2f(rc[w * REC + 2 * h] - M);
        L = fmaf(wt, rc[w * REC + 2 * h + 1], L);
        const f32x4 v = *reinterpret_cast<const f32x4*>(rc + w * REC + 4 + h * 256 + 4 * lane);
        o += wt * v;
      }
      const u32x4 ov = {nosent(o[0]), nosent(o[1]), nosent(o[2]), nosent(o[3])};
      __builtin_amdgcn_raw_buffer_store_b128(ov, rws, WS_P1 + (((g * NJ + h * 16 + (lane >> 2)) * NJ + j) * 64 + (lane & 3) * 16), 0, 16);
      if (lane == 0) __builtin_amdgcn_raw_buffer_store_b64(u32x2{nosent(M), nosent(L)}, rws, WS_ML + ((g * 2 + h) * NJ + j) * 8, 0, 16);
    }
    if (b < NRED && sw < 4) {
      u32x4 v[4]; unsigned spins = 0;
      const unsigned ybase = WS_Y + (b * NWG + sw * 64) * 64 + lane * 16;
      for (;;) {
        CBAR();
        bool ok = true;
#pragma unroll
        for (int i = 0; i < 4; ++i) v[i] = __builtin_amdgcn_raw_buffer_load_b128(rws, ybase + i * 1024, 0, 16);
#pragma unroll
        for (int i = 0; i < 4; ++i)
#pragma unroll
          for (int k = 0; k < 4; ++k) ok &= v[i][k] != SENT;
        if (__all(ok) || ++spins > SPIN_CAP) break;
      }
      if (sw == 0) STAMP(7);
      f32x4 s;
#pragma unroll
      for (int k = 0; k < 4; ++k)
        s[k] = red_q((__uint_as_float(v[0][k]) + __uint_as_float(v[1][k])) + (__uint_as_float(v[2][k]) + __uint_as_float(v[3][k])));
      float* fin = reinterpret_cast<float*>(&lds[L_FIN + 4]);
      if (lane < 4) *reinterpret_cast<f32x4*>(fin + sw * 16 + lane * 4) = s;
      unsigned old = 0;
      CBAR();
      if (lane == 0) old = __hip_atomic_fetch_add((LAS unsigned*)&lds[L_FIN], 1u, RLX_WG);
      old = __builtin_amdgcn_readfirstlane(old);
      CBAR();
      if (old == 3u) {
        if (lane < 16) out[b * 16 + lane] = (unsigned short)f2bf((fin[lane] + fin[16 + lane]) + (fin[32 + lane] + fin[48 + lane]));
        STAMP(8);
      }
    }
    return;
  }

  const unsigned tc0 = (unsigned)now();
#define WPACE(n) do { if (WP) { while ((unsigned)now() - tc0 < WT0 + (unsigned)(n) * WP) __builtin_amdgcn_s_sleep(4); __builtin_amdgcn_sched_barrier(0); } } while (0)
  f32x2 wof[16][2];
#define WO_LOAD() do { u32x2 wov[16]; \
    const auto ro = __builtin_amdgcn_make_buffer_rsrc((void*)wo, 0, (X_NOW & 4) ? 0 : HQ * DH * DM * 2, 0x00020000); \
    _Pragma("unroll") for (int r = 0; r < 16; ++r) { if ((r & 1) == 0) WPACE(20 * wave + r / 2); wov[r] = __builtin_amdgcn_raw_buffer_load_b64(ro, (g * 512 + j * 16 + r) * (DM * 2) + wave * 448 + lane * 8, 0, W_AUX); } \
    _Pragma("unroll") for (int r = 0; r < 16; ++r) _Pragma("unroll") for (int k = 0; k < 2; ++k) { wof[r][k] = f32x2{bflo(wov[r][k]), bfhi(wov[r][k])}; asm volatile("" : "+v"(wof[r][k])); } } while (0)
  if (wave == 1) {
    const u32x2 ep = __builtin_amdgcn_raw_buffer_load_b64(rws, WS_EPOCH, 0, 16);
    const unsigned t0 = (unsigned)now();
    if (lane == 0) { lds[L_CTR] = 0; lds[L_FIN] = 0; lds[L_FIN + 1] = 0; }
    __syncthreads();
    __builtin_amdgcn_sched_barrier(0);
    const int hc0 = (xg * 28 + (xi < 28 ? xi : 27)) * 8;
    const auto rx = __builtin_amdgcn_make_buffer_rsrc((void*)x, 0, DM * 2, 0x00020000);
    const auto rg = __builtin_amdgcn_make_buffer_rsrc((void*)gw, 0, DM * 2, 0x00020000);
    const auto rb2 = __builtin_amdgcn_make_buffer_rsrc((void*)b2, 0, DM * 2, 0x00020000);
    const auto r1 = __builtin_amdgcn_make_buffer_rsrc((void*)w1, 0, (xi < 28 && !(X_NOW & 1)) ? DM * FF * 2 : 0, 0x00020000);
    const auto r2 = __builtin_amdgcn_make_buffer_rsrc((void*)w2, 0, (xi < 28 && !(X_NOW & 2)) ? FF * DM * 2 : 0, 0x00020000);
    const auto rb = __builtin_amdgcn_make_buffer_rsrc((void*)b1, 0, FF * 2, 0x00020000);
    u32x4 w1v[7], wb[8]; unsigned short xr[7];
#pragma unroll
    for (int i = 0; i < 7; ++i) { WPACE(i); w1v[i] = __builtin_amdgcn_raw_buffer_load_b128(r1, (lane + 64 * i) * (FF * 2) + hc0 * 2, 0, W_AUX); xr[i] = x[lane + 64 * i]; }
    const u32x4 xv = __builtin_amdgcn_raw_buffer_load_b128(rx, lane * 16, 0, 0), gv = __builtin_amdgcn_raw_buffer_load_b128(rg, lane * 16, 0, 0);
    const u32x4 b2v = __builtin_amdgcn_raw_buffer_load_b128(rb2, lane * 16, 0, 0);
    const u32x4 bv = __builtin_amdgcn_raw_buffer_load_b128(rb, hc0 * 2, 0, 0);
#pragma unroll
    for (int i = 0; i < 8; ++i) { WPACE(7 + i); wb[i] = __builtin_amdgcn_raw_buffer_load_b128(r2, (hc0 + i) * (DM * 2) + lane * 16, 0, W_AUX); }
    if (!X_NOCLEAR) {
      const u32x4 sv = {SENT, SENT, SENT, SENT};
#pragma unroll
      for (int h = 0; h < 2; ++h) {
        WPACE(15 + h);
        __builtin_amdgcn_raw_buffer_store_b128(sv, rws, WS_P1 + (((g * NJ + h * 16 + (lane >> 2)) * NJ + j) * 64 + (lane & 3) * 16), 0, 16);
      }
      WPACE(17);
      if (lane < 2) __builtin_amdgcn_raw_buffer_store_b64(u32x2{SENT, SENT}, rws, WS_ML + ((g * 2 + lane) * NJ + j) * 8, 0, 16);
      if (lane < 56) {
        __builtin_amdgcn_raw_buffer_store_b128(sv, rws, WS_Y + (((lane >> 2) * NWG + b) * 64 + (lane & 3) * 16), 0, 16);
      }
      WPACE(18);
      if (lane < 56) __builtin_amdgcn_raw_buffer_store_b128(sv, rws, WS_Y + (((14 + (lane >> 2)) * NWG + b) * 64 + (lane & 3) * 16), 0, 16);
    }
    __builtin_amdgcn_sched_barrier(0);
    const unsigned long long tg = (((unsigned long long)ep[1] << 32) | ep[0]) + 1ull;
    const unsigned tlo = (unsigned)tg << 16, thi = (unsigned)(tg >> 16);
    asm volatile("s_waitcnt vmcnt(0)" ::: "memory");
    if (lane == 0) __builtin_amdgcn_raw_buffer_store_b64(u32x2{tlo, thi}, rws, WS_CL + b * 8, 0, 16);
    float s = 0.f;
#pragma unroll
    for (int k = 0; k < 4; ++k) { s = fmaf(bflo(xv[k]), bflo(gv[k]), s); s = fmaf(bfhi(xv[k]), bfhi(gv[k]), s); }
    s = red_all(s);
    const float gate = 1.f / (1.f + __expf(-s));
    const float bsc = b == NWG - 1 ? 1.f - gate : 0.f;
    f32x2 y2[4];
#pragma unroll
    for (int k = 0; k < 4; ++k) y2[k] = f32x2{bsc * bflo(b2v[k]), bsc * bfhi(b2v[k])};
    float hv[8];
#pragma unroll
    for (int k = 0; k < 8; ++k) hv[k] = 0.f;
#pragma unroll
    for (int i = 0; i < 7; ++i) {
      const float xf = bflo(xr[i]);
#pragma unroll
      for (int k = 0; k < 4; ++k) { hv[2 * k] = fmaf(xf, bflo(w1v[i][k]), hv[2 * k]); hv[2 * k + 1] = fmaf(xf, bfhi(w1v[i][k]), hv[2 * k + 1]); }
    }
#pragma unroll
    for (int k = 0; k < 8; ++k) {
      const float h = red_all(hv[k]) + ((k & 1) ? bfhi(bv[k >> 1]) : bflo(bv[k >> 1]));
      const float u = 0.7978845608028654f * (h + 0.044715f * h * h * h);
      const float ge = 0.5f * h * (1.f + tanhf(u)) * (1.f - gate);
#pragma unroll
      for (int k2 = 0; k2 < 4; ++k2) y2[k2] = __builtin_elementwise_fma(f32x2{ge, ge}, f32x2{bflo(wb[k][k2]), bfhi(wb[k][k2])}, y2[k2]);
    }
    float* yl = reinterpret_cast<float*>(&lds[L_Y2]);
    *reinterpret_cast<f32x4*>(yl + 8 * lane) = f32x4{y2[0][0], y2[0][1], y2[1][0], y2[1][1]};
    *reinterpret_cast<f32x4*>(yl + 8 * lane + 4) = f32x4{y2[2][0], y2[2][1], y2[3][0], y2[3][1]};
    if (lane == 0) yl[512] = gate;
    {
      unsigned spins = 0;
      WO_LOAD();
      while ((unsigned)now() - t0 < CHK_T) __builtin_amdgcn_s_sleep(64);
      for (;;) {
        CBAR();
        const u32x4 ga = __builtin_amdgcn_raw_buffer_load_b128(rws, WS_CL + lane * 32, 0, 16), gb = __builtin_amdgcn_raw_buffer_load_b128(rws, WS_CL + lane * 32 + 16, 0, 16);
        const bool ok = ga[0] == tlo && ga[1] == thi && ga[2] == tlo && ga[3] == thi && gb[0] == tlo && gb[1] == thi && gb[2] == tlo && gb[3] == thi;
        if (__all(ok) || ++spins > SPIN_CAP) break;
        __builtin_amdgcn_s_sleep(16);
      }
      if (b == 0 && lane == 0) __builtin_amdgcn_raw_buffer_store_b64(u32x2{(unsigned)tg, (unsigned)(tg >> 32)}, rws, WS_EPOCH, 0, 16);
    }
    STAMP(13);
    if (MODE != 1) STAMP(14);
  } else {
  STAMP(0);
  {
    const int qc0 = (xg * 32 + xi) * 16, pr = lane >> 1, hf = lane & 1;
    const auto rx = __builtin_amdgcn_make_buffer_rsrc((void*)x, 0, DM * 2, 0x00020000);
    const auto rq = __builtin_amdgcn_make_buffer_rsrc((void*)wq, 0, DM * HQ * DH * 2, 0x00020000);
    const u32x4 xa = __builtin_amdgcn_raw_buffer_load_b128(rx, pr * 28, 0, 0);
    const u32x3 xb = __builtin_amdgcn_raw_buffer_load_b96(rx, pr * 28 + 16, 0, 0);
    u32x4 w[14];
#pragma unroll
    for (int i = 0; i < 14; ++i) w[i] = __builtin_amdgcn_raw_buffer_load_b128(rq, (pr * 14) * (HQ * DH * 2) + (qc0 + 8 * hf) * 2, i * (HQ * DH * 2), W_AUX);
    const u32x2 ep = __builtin_amdgcn_raw_buffer_load_b64(rws, WS_EPOCH, 0, 16);
    __builtin_amdgcn_sched_barrier(0);
    const unsigned xw[7] = {xa[0], xa[1], xa[2], xa[3], xb[0], xb[1], xb[2]};
    f32x2 a[4];
#pragma unroll
    for (int k = 0; k < 4; ++k) a[k] = f32x2{0.f, 0.f};
#pragma unroll
    for (int i = 0; i < 14; ++i) {
      const float xv = (i & 1) ? bfhi(xw[i >> 1]) : bflo(xw[i >> 1]);
#pragma unroll
      for (int k = 0; k < 4; ++k) a[k] = __builtin_elementwise_fma(f32x2{xv, xv}, f32x2{bflo(w[i][k]), bfhi(w[i][k])}, a[k]);
    }
    float v[8] = {a[0][0], a[0][1], a[1][0], a[1][1], a[2][0], a[2][1], a[3][0], a[3][1]};
#pragma unroll
    for (int h = 0; h < 4; ++h) { const float t1 = v[h] + dpp<XOR8>(v[h]), t2 = v[h + 4] + dpp<XOR8>(v[h + 4]); v[h] = (lane & 8) ? t2 : t1; }
#pragma unroll
    for (int h = 0; h < 2; ++h) { const float t1 = v[h] + dpp<ROR12>(v[h]), t2 = v[h + 2] + dpp<ROR4>(v[h + 2]); v[h] = (lane & 4) ? t2 : t1; }
    { const float t1 = v[0] + dpp<XOR2>(v[0]), t2 = v[1] + dpp<XOR2>(v[1]); v[0] = (lane & 2) ? t2 : t1; }
    const float qv = sw32_add(sw16_add(v[0]));
    const unsigned long long tg = (((unsigned long long)ep[1] << 32) | ep[0]) + 1ull;
    const unsigned tlo = (unsigned)tg << 16, thi = (unsigned)(tg >> 16);
    if (lane < 16) {
      const int e = ((lane >> 3) & 1) * 4 + ((lane >> 2) & 1) * 2 + ((lane >> 1) & 1);
      __builtin_amdgcn_raw_buffer_store_b64(u32x2{tlo | f2bf(qv), thi}, rws, WS_QG + (qc0 + 8 * hf + e) * 8, 0, 16);
    }
    STAMP(1);
    {
      u32x4 gq[4]; unsigned spins = 0;
      for (;;) {
        CBAR();
        bool ok = true;
#pragma unroll
        for (int i = 0; i < 4; ++i) gq[i] = __builtin_amdgcn_raw_buffer_load_b128(rws, WS_QG + (g * 512 + 8 * lane + 2 * i) * 8, 0, 16);
#pragma unroll
        for (int i = 0; i < 4; ++i) ok &= gq[i][1] == thi && gq[i][3] == thi && (gq[i][0] & 0xffff0000u) == tlo && (gq[i][2] & 0xffff0000u) == tlo;
        if (STAMPS && spins == 0) STAMP(25);
        if (__all(ok) || ++spins > SPIN_CAP) break;
      }
      if (STAMPS && lane == 0) reinterpret_cast<unsigned long long*>(ws + WS_STAMP)[b * 32 + 26] = spins + 1;
      u32x4 pk;
#pragma unroll
      for (int i = 0; i < 4; ++i) pk[i] = (gq[i][0] & 0xffffu) | (gq[i][2] << 16);
      *reinterpret_cast<u32x4*>(&lds[L_Q + 4 * lane]) = pk;
    }
    STAMP(15);
    __syncthreads();
    STAMP(2);
  }
    WO_LOAD();
  }
  __syncthreads();
  if (wave == 0) STAMP(4);
  if (STAMPS && wave == 1 && lane == 0) { unsigned long long* sp = reinterpret_cast<unsigned long long*>(ws + WS_STAMP) + b * 32; sp[9] = (unsigned)__builtin_amdgcn_s_getreg(20 | (3 << 11)) & 0xfu; sp[10] = lds[L_CTR] - NSW; sp[11] = lds[L_FIN + 1]; }
  {
    const float* yl = reinterpret_cast<const float*>(&lds[L_Y2]);
    const f32x4 ya = *reinterpret_cast<const f32x4*>(yl + 224 * wave + 4 * lane);
    const float gate = yl[512];
    u32x4 pa, pb; u32x2 ma, mb; unsigned spins = 0;
    const unsigned pbase = WS_P1 + (g * NJ + j) * NJ * 64 + lane * 16, mbase = WS_ML + ((g * 2 + (j >> 4)) * NJ + (lane >> 2)) * 8;
    for (;;) {
      CBAR();
      pa = __builtin_amdgcn_raw_buffer_load_b128(rws, pbase, 0, 16);
      pb = __builtin_amdgcn_raw_buffer_load_b128(rws, pbase + 1024, 0, 16);
      ma = __builtin_amdgcn_raw_buffer_load_b64(rws, mbase, 0, 16);
      mb = __builtin_amdgcn_raw_buffer_load_b64(rws, mbase + 128, 0, 16);
      bool ok = ma[0] != SENT && ma[1] != SENT && mb[0] != SENT && mb[1] != SENT;
#pragma unroll
      for (int k = 0; k < 4; ++k) ok &= pa[k] != SENT && pb[k] != SENT;
      if (__all(ok) || ++spins > SPIN_CAP) break;
    }
    if (wave == 0) STAMP(5);
    const float m_a = __uint_as_float(ma[0]), m_b = __uint_as_float(mb[0]);
    float M = fmaxf(m_a, m_b);
    M = fmaxf(M, dpp<ROR4>(M)); M = fmaxf(M, dpp<XOR8>(M)); M = sw32_max(sw16_max(M));
    const float wa = __builtin_amdgcn_exp2f(m_a - M), wb = __builtin_amdgcn_exp2f(m_b - M);
    const float L = red_q(wa * __uint_as_float(ma[1]) + wb * __uint_as_float(mb[1]));
    const float sc = gate / L;
    float o[4];
#pragma unroll
    for (int k = 0; k < 4; ++k) o[k] = red_q(wa * __uint_as_float(pa[k]) + wb * __uint_as_float(pb[k]));
    f32x2 y[2] = {f32x2{0.f, 0.f}, f32x2{0.f, 0.f}};
#define YROW(qd, k) { const float ov = dpp<(qd) * 0x55>(o[k]); _Pragma("unroll") for (int k2 = 0; k2 < 2; ++k2) \
      y[k2] = __builtin_elementwise_fma(f32x2{ov, ov}, wof[4 * (qd) + (k)][k2], y[k2]); }
#define YQ(qd) YROW(qd, 0) YROW(qd, 1) YROW(qd, 2) YROW(qd, 3)
    YQ(0) YQ(1) YQ(2) YQ(3)
    if (lane < 56)
      __builtin_amdgcn_raw_buffer_store_b128(u32x4{nosent(fmaf(sc, y[0][0], ya[0])), nosent(fmaf(sc, y[0][1], ya[1])), nosent(fmaf(sc, y[1][0], ya[2])), nosent(fmaf(sc, y[1][1], ya[3]))},
                                             rws, WS_Y + (((wave * 14 + (lane >> 2)) * NWG + b) * 64 + (lane & 3) * 16), 0, 16);
    if (wave == 0) STAMP(6);
  }
}

extern "C" void kernel_launch(void* const* d_in, const int* in_sizes, int n_in, void* d_out, int out_size, void* d_ws, size_t ws_size, hipStream_t stream) {
  k_fused<<<NWG, NTHR, 0, stream>>>((const unsigned short*)d_in[0], (const unsigned char*)d_in[1], (const unsigned char*)d_in[2], (const unsigned short*)d_in[3],
                                    (const unsigned short*)d_in[4], (const unsigned short*)d_in[6], (const unsigned short*)d_in[7], (const unsigned short*)d_in[8],
                                    (const unsigned short*)d_in[9], (const unsigned short*)d_in[10], (unsigned short*)d_out, (unsigned char*)d_ws);
}
```

```cpp
#include <hip/hip_runtime.h>
#include <hip/hip_bf16.h>
#include <cstdint>

#define NC 4
#define HD 5
#define RT 16
#define GL 4
#define WIN 20
#define WHI WIN
#define TSW 224
#define W0 WIN
#define TS0 0
#define LPRIO 0
#define CSLEEP 1
#define X_NOQ 0
#define X_NOTAIL 0
#define PSLEEP 0
#define QPOLL_T 300
#define QSLEEP 8
#define START_T 0
#define WP 0
#define WT0 900
#define W_AUX 2
#define X_NOW 0
#define STAMPS 0

constexpr int DM = 448, HQ = 16, HKV = 8, SK = 65536, DH = 256, FF = 1792;
constexpr int NWG = 256, NJ = 32, NWAVE = NC + 3, NTHR = NWAVE * 64;
constexpr int TILE_BYTES = 4096;
constexpr int RANGE_TILES = SK / 8 / NJ;
static_assert(NC >= 4 && RT % 4 == 0 && RANGE_TILES % NC == 0 && HD >= 1 && HD <= 6 && (GL == 2 || GL == 4) && WIN % GL == 0 && WIN <= 8 * RT - 8, "deal");
constexpr int NRED = 28;
constexpr unsigned SPIN_CAP = 1u << 20;

constexpr unsigned WS_EPOCH = 0, WS_QG = 8192, WS_ML = WS_QG + 4096 * 8, WS_P1 = WS_ML + HQ * NJ * 16,
                   WS_Y = WS_P1 + HKV * NJ * NJ * 64, WS_END = WS_Y + NRED * NWG * 64, WS_STAMP = 2u << 20;
constexpr int L_Q = RT * 2048, L_FULL = L_Q + 256, L_QRDY = L_FULL + 1, L_FIN = L_FULL + 2, L_FREE = L_FULL + 4, L_TAG = L_FULL + 20, L_FINV = L_FULL + 24, L_Y2 = L_FINV + 64,
              L_REC = L_Y2 + 520, REC = 516, L_END = L_REC + NC * REC;
static_assert(L_END * 4 <= 160 * 1024 && NC <= 16, "LDS");

using bf16x2 = __attribute__((ext_vector_type(2))) __bf16;
using u32x2  = __attribute__((ext_vector_type(2))) unsigned;
using u32x3  = __attribute__((ext_vector_type(3))) unsigned;
using u32x4  = __attribute__((ext_vector_type(4))) unsigned;
using f32x2  = __attribute__((ext_vector_type(2))) float;
using f32x4  = __attribute__((ext_vector_type(4))) float;
#define LAS __attribute__((address_space(3)))
#define RLX_WG __ATOMIC_RELAXED, __HIP_MEMORY_SCOPE_WORKGROUP
#define CBAR() asm volatile("" ::: "memory")
#define SB() __builtin_amdgcn_sched_barrier(0)
#define LGKM0() asm volatile("s_waitcnt lgkmcnt(0)" ::: "memory")
#define STR2(x) #x
#define STR(x) STR2(x)

__device__ __forceinline__ float dot2(unsigned k, unsigned q, float acc) {
  return __builtin_amdgcn_fdot2_f32_bf16(__builtin_bit_cast(bf16x2, k), __builtin_bit_cast(bf16x2, q), acc, false);
}
template <int CTRL> __device__ __forceinline__ float dpp(float x) {
  return __builtin_bit_cast(float, __builtin_amdgcn_mov_dpp(__builtin_bit_cast(int, x), CTRL, 0xf, 0xf, true));
}
constexpr int XOR1 = 0xB1, XOR2 = 0x4E, XOR7 = 0x141, XOR8 = 0x128, ROR4 = 0x124, ROR12 = 0x12C;
__device__ __forceinline__ float bflo(unsigned u) { return __builtin_bit_cast(float, u << 16); }
__device__ __forceinline__ float bfhi(unsigned u) { return __builtin_bit_cast(float, u & 0xffff0000u); }
__device__ __forceinline__ unsigned f2bf(float f) { __hip_bfloat16 b = __float2bfloat16(f); return __builtin_bit_cast(unsigned short, b); }
__device__ __forceinline__ float sw16_add(float x) { auto s = __builtin_amdgcn_permlane16_swap(__float_as_uint(x), __float_as_uint(x), false, false); return __uint_as_float(s[0]) + __uint_as_float(s[1]); }
__device__ __forceinline__ float sw32_add(float x) { auto s = __builtin_amdgcn_permlane32_swap(__float_as_uint(x), __float_as_uint(x), false, false); return __uint_as_float(s[0]) + __uint_as_float(s[1]); }
__device__ __forceinline__ float sw16_max(float x) { auto s = __builtin_amdgcn_permlane16_swap(__float_as_uint(x), __float_as_uint(x), false, false); return fmaxf(__uint_as_float(s[0]), __uint_as_float(s[1])); }
__device__ __forceinline__ float sw32_max(float x) { auto s = __builtin_amdgcn_permlane32_swap(__float_as_uint(x), __float_as_uint(x), false, false); return fmaxf(__uint_as_float(s[0]), __uint_as_float(s[1])); }
__device__ __forceinline__ float red_q(float x) { x += dpp<ROR4>(x); x += dpp<XOR8>(x); x = sw16_add(x); return sw32_add(x); }
__device__ __forceinline__ float red_all(float x) { x += dpp<XOR1>(x); x += dpp<XOR2>(x); return red_q(x); }
template <int CTRL> __device__ __forceinline__ unsigned dppu(unsigned x) { return (unsigned)__builtin_amdgcn_mov_dpp((int)x, CTRL, 0xf, 0xf, true); }
__device__ __forceinline__ unsigned rotl(unsigned x, unsigned r) { return __builtin_amdgcn_alignbit(x, x, (32u - r) & 31u); }
__device__ __forceinline__ unsigned rec_fold(u32x4 w, unsigned r8) {
  unsigned h = rotl(w[0] ^ rotl(w[1], 2) ^ rotl(w[2], 4) ^ rotl(w[3], 6), r8);
  h ^= dppu<XOR1>(h); h ^= dppu<XOR2>(h);
  return h;
}
__device__ __forceinline__ u32x4 rec_seal(f32x4 v, unsigned tag, unsigned r8) {
  u32x4 w = {__float_as_uint(v[0]) & ~3u, __float_as_uint(v[1]) & ~3u, __float_as_uint(v[2]) & ~3u, __float_as_uint(v[3]) & ~3u};
  const unsigned cb = (rec_fold(w, r8) ^ tag) >> r8;
  w[0] |= cb & 3u; w[1] |= (cb >> 2) & 3u; w[2] |= (cb >> 4) & 3u; w[3] |= (cb >> 6) & 3u;
  return w;
}
__device__ __forceinline__ float rec_val(unsigned w) { return __uint_as_float(w & ~3u); }
__device__ __forceinline__ unsigned long long now() { return __builtin_amdgcn_s_memrealtime(); }
__device__ __forceinline__ unsigned lds_peek(unsigned byte_addr) {
  unsigned v;
  asm volatile("ds_read_b32 %0, %1\n\ts_waitcnt lgkmcnt(0)" : "=v"(v) : "v"(byte_addr) : "memory");
  return __builtin_amdgcn_readfirstlane(v);
}
__device__ __forceinline__ void lds_poke(unsigned byte_addr, unsigned v) { asm volatile("ds_write_b32 %0, %1" :: "v"(byte_addr), "v"(v) : "memory"); }
#define STAMP(i) do { if (STAMPS) { if (lane == 0) reinterpret_cast<unsigned long long*>(ws + WS_STAMP)[b * 32 + (i)] = now(); } } while (0)

__global__ void __launch_bounds__(NTHR, (NWAVE + 3) / 4)
k_fused(const unsigned short* __restrict__ x, const unsigned char* __restrict__ Kc, const unsigned char* __restrict__ Vc,
        const unsigned short* __restrict__ wq, const unsigned short* __restrict__ wo, const unsigned short* __restrict__ w1,
        const unsigned short* __restrict__ b1, const unsigned short* __restrict__ w2, const unsigned short* __restrict__ b2,
        const unsigned short* __restrict__ gw, unsigned short* __restrict__ out, unsigned char* ws) {
  __shared__ __attribute__((aligned(16))) unsigned lds[L_END];
  const unsigned lbase = (unsigned)(size_t)(LAS unsigned*)lds;
  const int b = blockIdx.x, g = b >> 5, j = b & 31;
  const int tid = threadIdx.x, lane = tid & 63, wave = __builtin_amdgcn_readfirstlane(tid >> 6);
  const auto rws = __builtin_amdgcn_make_buffer_rsrc((void*)ws, 0, (int)WS_END, 0x00020000);
  const int xg = b & 7, xi = b >> 3;

  if (wave == 2) {
    const auto rk = __builtin_amdgcn_make_buffer_rsrc((void*)Kc, 0, HKV * SK * DH * 2, 0x00020000);
    const auto rv = __builtin_amdgcn_make_buffer_rsrc((void*)Vc, 0, HKV * SK * DH * 2, 0x00020000);
    if (LPRIO) __builtin_amdgcn_s_setprio(LPRIO);
    const unsigned voff = lane * 16;
    unsigned off = (unsigned)b * (unsigned)(RANGE_TILES * TILE_BYTES);
    if (lane < 24) lds_poke(lbase + (L_FULL + lane) * 4, 0u);
    LGKM0();
    if (START_T) { const unsigned t0 = (unsigned)now(); while ((unsigned)now() - t0 < START_T) __builtin_amdgcn_s_sleep(1); }
#define DMA(R, S, O) __builtin_amdgcn_raw_ptr_buffer_load_lds(R, (LAS void*)((LAS unsigned char*)lds + (S)), 16, voff, off, O, 2)
    int issued = 0;
    int lmax = 0;
    unsigned nboost = 0;
    const unsigned fulla = lbase + L_FULL * 4, ctla = lbase + (L_FREE + (lane < NC ? lane : 0)) * 4;
#define PUB(W) do { asm volatile("s_waitcnt vmcnt(" STR(W - GL) ")" ::: "memory"); \
      lmax = max(lmax, issued - (W - GL)); lds_poke(fulla, lmax); issued += GL; } while (0)
#define PART(W, R, S) do { PUB(W); DMA(R, S, 0); DMA(R, S, 1024); if (GL == 2) PUB(W); DMA(R, S, 2048); DMA(R, S, 3072); } while (0)
#define TILE(W) do { PART(W, rk, slot); PART(W, rv, slot + TILE_BYTES); off += TILE_BYTES; slot += 2 * TILE_BYTES; } while (0)
#define BLOCK(W) do { TILE(W); TILE(W); TILE(W); TILE(W); } while (0)
    unsigned slot = 0;
    TILE(W0); TILE(W0);
    __builtin_amdgcn_s_barrier();
    TILE(W0); TILE(W0);
    for (int t = 4; t < RANGE_TILES; t += 4) {
      if (slot == RT * 2 * TILE_BYTES) slot = 0;
      {
        const unsigned need = t + 4 > RT ? (unsigned)(t + 4 - RT + NC - 1) / NC : 0u;
        unsigned v;
        asm volatile("ds_read_b32 %0, %1\n\ts_waitcnt lgkmcnt(0)" : "=v"(v) : "v"(ctla) : "memory");
        if (__any(v < need)) {
          asm volatile("s_waitcnt vmcnt(0)" ::: "memory");
          lmax = issued; lds_poke(fulla, issued);
          unsigned spins = 0;
          do { __builtin_amdgcn_s_sleep(1); asm volatile("ds_read_b32 %0, %1\n\ts_waitcnt lgkmcnt(0)" : "=v"(v) : "v"(ctla) : "memory"); } while (__any(v < need) && ++spins < SPIN_CAP);
          nboost += spins << 16;
        }
      }
      if (WHI != WIN && t >= TSW) BLOCK(WHI); else if (W0 != WIN && t < TS0) BLOCK(W0); else BLOCK(WIN);
      if (STAMPS && (t & 31) == 0) STAMP(16 + (t >> 5));
    }
#define TAILPUB(n) if (WHI > (n)) { asm volatile("s_waitcnt vmcnt(" #n ")" ::: "memory"); lds_poke(lbase + L_FULL * 4, RANGE_TILES * 8 - (n)); }
    TAILPUB(56) TAILPUB(52) TAILPUB(48) TAILPUB(44) TAILPUB(40) TAILPUB(36) TAILPUB(32) TAILPUB(28) TAILPUB(24) TAILPUB(20) TAILPUB(16) TAILPUB(12) TAILPUB(8) TAILPUB(4) TAILPUB(0)
    if (STAMPS) lds_poke(lbase + (L_FIN + 1) * 4, nboost);
    LGKM0();
    STAMP(24);
    __builtin_amdgcn_s_barrier();
    return;
  }

  if (wave >= 3) {
    const int sw = wave - 3, c = lane & 31, t = lane & 15;
    constexpr float C2 = 0.0625f * 1.4426950408889634f;
    __builtin_amdgcn_s_barrier();
    u32x4 q0, q1;
    float m = -INFINITY, l = 0.f;
    f32x2 acc0[4], acc1[4];
#pragma unroll
    for (int k = 0; k < 4; ++k) { acc0[k] = f32x2{0.f, 0.f}; acc1[k] = f32x2{0.f, 0.f}; }
    float x_[8], p;
    unsigned full = 0, tag;
    const unsigned r8 = 8 * (lane & 3);
#define WAITFULL(n) do { unsigned spins = 0; while (full < (unsigned)(n) && spins < SPIN_CAP) { full = lds_peek(lbase + L_FULL * 4); if (full >= (unsigned)(n)) break; ++spins; __builtin_amdgcn_s_sleep(CSLEEP); } } while (0)
#define RD4(dst, tile, part) do { const u32x4* src_ = reinterpret_cast<const u32x4*>(&lds[(((tile) % RT) * 2 + (part)) * 1024 + lane * 4]); \
      CBAR(); dst[0] = src_[0]; dst[1] = src_[64]; dst[2] = src_[128]; dst[3] = src_[192]; } while (0)
#define PIN4(a) asm volatile("" : "+v"(a[0]), "+v"(a[1]), "+v"(a[2]), "+v"(a[3]) :: "memory")
#define RELEASE(n) do { LGKM0(); lds_poke(lbase + (L_FREE + sw) * 4, (n)); } while (0)
#define QK1(KK, i) { float a_ = 0.f, b_ = 0.f; \
      _Pragma("unroll") for (int k = 0; k < 4; ++k) { a_ = dot2((KK)[k], q0[k], a_); b_ = dot2((KK)[k], q1[k], b_); } \
      x_[2 * (i)] = a_; x_[2 * (i) + 1] = b_; }
#define TR_STEP(HALF, CTRL, BIT) _Pragma("unroll") for (int h = 0; h < (HALF); ++h) { \
      const float lo_ = x_[h], hi_ = x_[h + (HALF)]; const float t1_ = lo_ + dpp<CTRL>(lo_), t2_ = hi_ + dpp<CTRL>(hi_); \
      x_[h] = (t & (BIT)) ? t2_ : t1_; }
#define SOFTMAX() { TR_STEP(4, XOR8, 8) TR_STEP(2, XOR7, 4) TR_STEP(1, XOR2, 2) x_[0] += dpp<XOR1>(x_[0]); \
      const float sc = sw16_add(x_[0]) * C2; \
      if (__any(sc > m)) {                                       \
        float pm = sc; pm = fmaxf(pm, dpp<ROR4>(pm)); pm = fmaxf(pm, dpp<XOR8>(pm)); pm = sw32_max(pm); \
        const float mn = fmaxf(m, pm), alpha = __builtin_amdgcn_exp2f(m - mn); \
        m = mn; l *= alpha; \
        const float a0 = dpp<0x00>(alpha), a1 = dpp<0xAA>(alpha); \
        _Pragma("unroll") for (int k = 0; k < 4; ++k) { acc0[k] *= a0; acc1[k] *= a1; } } \
      p = __builtin_amdgcn_exp2f(sc - m); l += p; asm volatile("" : "+v"(p)); }
#define PV1(VV, i) { const float p0 = dpp<0x150 + 4 * (i)>(p), p1 = dpp<0x150 + 4 * (i) + 2>(p); \
      _Pragma("unroll") for (int k = 0; k < 4; ++k) { const f32x2 vf = {bflo((VV)[k]), bfhi((VV)[k])}; \
        acc0[k] = __builtin_elementwise_fma(f32x2{p0, p0}, vf, acc0[k]); acc1[k] = __builtin_elementwise_fma(f32x2{p1, p1}, vf, acc1[k]); } }
#define PINACC() _Pragma("unroll") for (int k = 0; k < 4; ++k) { asm volatile("" : "+v"(acc0[k])); asm volatile("" : "+v"(acc1[k])); }
    {
      u32x4 Kb[HD][4], Vb[HD][4];
#pragma unroll
      for (int d = 0; d < HD; ++d) {
        const int tile = d * NC + sw;
        WAITFULL(tile * 8 + 8);
        RD4(Kb[d], tile, 0); RD4(Vb[d], tile, 1);
        RELEASE(d + 1);
        PIN4(Kb[d]); PIN4(Vb[d]);
      }
      { unsigned spins = 0; while (lds_peek(lbase + L_QRDY * 4) == 0u && ++spins < SPIN_CAP) __builtin_amdgcn_s_sleep(1); }
      q0 = *reinterpret_cast<const u32x4*>(&lds[L_Q + 4 * c]); q1 = *reinterpret_cast<const u32x4*>(&lds[L_Q + 128 + 4 * c]);
      tag = lds_peek(lbase + L_TAG * 4);
#pragma unroll
      for (int d = 0; d < HD; ++d) {
        QK1(Kb[d][0], 0) QK1(Kb[d][1], 1) QK1(Kb[d][2], 2) QK1(Kb[d][3], 3)
        SOFTMAX();
        PV1(Vb[d][0], 0) PV1(Vb[d][1], 1) PV1(Vb[d][2], 2) PV1(Vb[d][3], 3)
        PINACC(); SB();
      }
    }
    if (sw == 0) STAMP(25);
    for (int n = HD; n < RANGE_TILES / NC; ++n) {
      const int tile = n * NC + sw;
      u32x4 Kb[4], Vb[4];
      WAITFULL(tile * 8 + 4);
      RD4(Kb, tile, 0);
      QK1(Kb[0], 0) QK1(Kb[1], 1) QK1(Kb[2], 2) QK1(Kb[3], 3)
      SOFTMAX();
      WAITFULL(tile * 8 + 8);
      RD4(Vb, tile, 1);
      RELEASE(n + 1);
      PIN4(Vb);
      PV1(Vb[0], 0) PV1(Vb[1], 1) PV1(Vb[2], 2) PV1(Vb[3], 3)
      PINACC(); SB();
    }
    if (sw == NC - 1) STAMP(3);
    float r[8];
#pragma unroll
    for (int k = 0; k < 4; ++k)
#pragma unroll
      for (int e = 0; e < 2; ++e) {
        auto s = __builtin_amdgcn_permlane32_swap(__float_as_uint(acc0[k][e]), __float_as_uint(acc1[k][e]), false, false);
        r[2 * k + e] = __uint_as_float(s[0]) + __uint_as_float(s[1]);
      }
    l += dpp<ROR4>(l); l += dpp<XOR8>(l); l = sw32_add(l);
    float* rec = reinterpret_cast<float*>(&lds[L_REC + sw * REC]);
    *reinterpret_cast<f32x4*>(rec + 4 + 8 * lane) = f32x4{r[0], r[1], r[2], r[3]};
    *reinterpret_cast<f32x4*>(rec + 8 + 8 * lane) = f32x4{r[4], r[5], r[6], r[7]};
    if (lane == 0 || lane == 2) { rec[lane ? 2 : 0] = m; rec[lane ? 3 : 1] = l; }
    __syncthreads();
    if (X_NOTAIL) { if (l == 123.456f) out[0] = 1; return; }
    if (sw < 2) {
      const int h = sw;
      const float* rc = reinterpret_cast<const float*>(&lds[L_REC]);
      float M = -INFINITY;
#pragma unroll
      for (int w = 0; w < NC; ++w) M = fmaxf(M, rc[w * REC + 2 * h]);
      float L = 0.f; f32x4 o = {0.f, 0.f, 0.f, 0.f};
#pragma unroll
      for (int w = 0; w < NC; ++w) {
        const float wt = __builtin_amdgcn_exp2f(rc[w * REC + 2 * h] - M);
        L = fmaf(wt, rc[w * REC + 2 * h + 1], L);
        const f32x4 v = *reinterpret_cast<const f32x4*>(rc + w * REC + 4 + h * 256 + 4 * lane);
        o += wt * v;
      }
      __builtin_amdgcn_raw_buffer_store_b128(rec_seal(o, tag, r8), rws, WS_P1 + (((g * NJ + h * 16 + (lane >> 2)) * NJ + j) * 64 + (lane & 3) * 16), 0, 16);
      if (lane == 0) __builtin_amdgcn_raw_buffer_store_b128(u32x4{__float_as_uint(M), __float_as_uint(L), __float_as_uint(M) ^ rotl(__float_as_uint(L), 16) ^ tag, 0u}, rws, WS_ML + ((g * 2 + h) * NJ + j) * 16, 0, 16);
    }
    if (b < NRED && sw < 4) {
      u32x4 v[4]; unsigned spins = 0;
      const unsigned ybase = WS_Y + (b * NWG + sw * 64) * 64 + lane * 16;
      for (;;) {
        CBAR();
#pragma unroll
        for (int i = 0; i < 4; ++i) v[i] = __builtin_amdgcn_raw_buffer_load_b128(rws, ybase + i * 1024, 0, 16);
        const bool ok = (((rec_fold(v[0], r8) ^ tag) | (rec_fold(v[1], r8) ^ tag)) | ((rec_fold(v[2], r8) ^ tag) | (rec_fold(v[3], r8) ^ tag))) == 0u;
        if (__all(ok) || ++spins > SPIN_CAP) break;
        if (PSLEEP) __builtin_amdgcn_s_sleep(PSLEEP);
      }
      if (sw == 0) STAMP(7);
      f32x4 s;
#pragma unroll
      for (int k = 0; k < 4; ++k)
        s[k] = red_q((rec_val(v[0][k]) + rec_val(v[1][k])) + (rec_val(v[2][k]) + rec_val(v[3][k])));
      float* fin = reinterpret_cast<float*>(&lds[L_FINV]);
      if (lane < 4) *reinterpret_cast<f32x4*>(fin + sw * 16 + lane * 4) = s;
      unsigned old = 0;
      CBAR();
      if (lane == 0) old = __hip_atomic_fetch_add((LAS unsigned*)&lds[L_FIN], 1u, RLX_WG);
      old = __builtin_amdgcn_readfirstlane(old);
      CBAR();
      if (old == 3u) {
        if (lane < 16) out[b * 16 + lane] = (unsigned short)f2bf((fin[lane] + fin[16 + lane]) + (fin[32 + lane] + fin[48 + lane]));
        if (b == 0 && lane == 0) __builtin_amdgcn_raw_buffer_store_b64(u32x2{lds[L_TAG + 1], lds[L_TAG + 2]}, rws, WS_EPOCH, 0, 16);
        STAMP(8);
      }
    }
    return;
  }

  const unsigned tc0 = (unsigned)now();
#define WPACE(n) do { if (WP) { while ((unsigned)now() - tc0 < WT0 + (unsigned)(n) * WP) __builtin_amdgcn_s_sleep(4); SB(); } } while (0)
  f32x2 wof[16][2];
#define WO_LOAD() do { u32x2 wov[16]; \
    const auto ro = __builtin_amdgcn_make_buffer_rsrc((void*)wo, 0, (X_NOW & 4) ? 0 : HQ * DH * DM * 2, 0x00020000); \
    _Pragma("unroll") for (int r = 0; r < 16; ++r) { if ((r & 1) == 0) WPACE(20 * wave + r / 2); wov[r] = __builtin_amdgcn_raw_buffer_load_b64(ro, (g * 512 + j * 16 + r) * (DM * 2) + wave * 448 + lane * 8, 0, W_AUX); } \
    _Pragma("unroll") for (int r = 0; r < 16; ++r) _Pragma("unroll") for (int k = 0; k < 2; ++k) { wof[r][k] = f32x2{bflo(wov[r][k]), bfhi(wov[r][k])}; asm volatile("" : "+v"(wof[r][k])); } } while (0)
  if (wave == 1) {
    __builtin_amdgcn_s_barrier();
    { unsigned spins = 0; while (lds_peek(lbase + L_QRDY * 4) == 0u && ++spins < SPIN_CAP) __builtin_amdgcn_s_sleep(4); }
    const int hc0 = (xg * 28 + (xi < 28 ? xi : 27)) * 8;
    const auto rx = __builtin_amdgcn_make_buffer_rsrc((void*)x, 0, DM * 2, 0x00020000);
    const auto rg = __builtin_amdgcn_make_buffer_rsrc((void*)gw, 0, DM * 2, 0x00020000);
    const auto rb2 = __builtin_amdgcn_make_buffer_rsrc((void*)b2, 0, DM * 2, 0x00020000);
    const auto r1 = __builtin_amdgcn_make_buffer_rsrc((void*)w1, 0, (xi < 28 && !(X_NOW & 1)) ? DM * FF * 2 : 0, 0x00020000);
    const auto r2 = __builtin_amdgcn_make_buffer_rsrc((void*)w2, 0, (xi < 28 && !(X_NOW & 2)) ? FF * DM * 2 : 0, 0x00020000);
    const auto rb = __builtin_amdgcn_make_buffer_rsrc((void*)b1, 0, FF * 2, 0x00020000);
    u32x4 w1v[7], wb[8]; unsigned short xr[7];
#pragma unroll
    for (int i = 0; i < 7; ++i) { WPACE(i); w1v[i] = __builtin_amdgcn_raw_buffer_load_b128(r1, (lane + 64 * i) * (FF * 2) + hc0 * 2, 0, W_AUX); xr[i] = x[lane + 64 * i]; }
    const u32x4 xv = __builtin_amdgcn_raw_buffer_load_b128(rx, lane * 16, 0, 0), gv = __builtin_amdgcn_raw_buffer_load_b128(rg, lane * 16, 0, 0);
    const u32x4 b2v = __builtin_amdgcn_raw_buffer_load_b128(rb2, lane * 16, 0, 0);
    const u32x4 bv = __builtin_amdgcn_raw_buffer_load_b128(rb, hc0 * 2, 0, 0);
#pragma unroll
    for (int i = 0; i < 8; ++i) { WPACE(7 + i); wb[i] = __builtin_amdgcn_raw_buffer_load_b128(r2, (hc0 + i) * (DM * 2) + lane * 16, 0, W_AUX); }
    float s = 0.f;
#pragma unroll
    for (int k = 0; k < 4; ++k) { s = fmaf(bflo(xv[k]), bflo(gv[k]), s); s = fmaf(bfhi(xv[k]), bfhi(gv[k]), s); }
    s = red_all(s);
    const float gate = 1.f / (1.f + __expf(-s));
    const float bsc = b == NWG - 1 ? 1.f - gate : 0.f;
    f32x2 y2[4];
#pragma unroll
    for (int k = 0; k < 4; ++k) y2[k] = f32x2{bsc * bflo(b2v[k]), bsc * bfhi(b2v[k])};
    float hv[8];
#pragma unroll
    for (int k = 0; k < 8; ++k) hv[k] = 0.f;
#pragma unroll
    for (int i = 0; i < 7; ++i) {
      const float xf = bflo(xr[i]);
#pragma unroll
      for (int k = 0; k < 4; ++k) { hv[2 * k] = fmaf(xf, bflo(w1v[i][k]), hv[2 * k]); hv[2 * k + 1] = fmaf(xf, bfhi(w1v[i][k]), hv[2 * k + 1]); }
    }
#pragma unroll
    for (int k = 0; k < 8; ++k) {
      const float h = red_all(hv[k]) + ((k & 1) ? bfhi(bv[k >> 1]) : bflo(bv[k >> 1]));
      const float u = 0.7978845608028654f * (h + 0.044715f * h * h * h);
      const float ge = 0.5f * h * (1.f + tanhf(u)) * (1.f - gate);
#pragma unroll
      for (int k2 = 0; k2 < 4; ++k2) y2[k2] = __builtin_elementwise_fma(f32x2{ge, ge}, f32x2{bflo(wb[k][k2]), bfhi(wb[k][k2])}, y2[k2]);
    }
    float* yl = reinterpret_cast<float*>(&lds[L_Y2]);
    *reinterpret_cast<f32x4*>(yl + 8 * lane) = f32x4{y2[0][0], y2[0][1], y2[1][0], y2[1][1]};
    *reinterpret_cast<f32x4*>(yl + 8 * lane + 4) = f32x4{y2[2][0], y2[2][1], y2[3][0], y2[3][1]};
    if (lane == 0) yl[512] = gate;
    WO_LOAD();
    STAMP(13);
  } else {
    STAMP(0);
    const int qc0 = (xg * 32 + xi) * 16, pr = lane >> 1, hf = lane & 1;
    const auto rx = __builtin_amdgcn_make_buffer_rsrc((void*)x, 0, DM * 2, 0x00020000);
    const auto rq = __builtin_amdgcn_make_buffer_rsrc((void*)wq, 0, (X_NOQ & 1) ? 0 : DM * HQ * DH * 2, 0x00020000);
    const u32x4 xa = __builtin_amdgcn_raw_buffer_load_b128(rx, pr * 28, 0, 0);
    const u32x3 xb = __builtin_amdgcn_raw_buffer_load_b96(rx, pr * 28 + 16, 0, 0);
    u32x4 w[14];
#pragma unroll
    for (int i = 0; i < 14; ++i) w[i] = __builtin_amdgcn_raw_buffer_load_b128(rq, (pr * 14) * (HQ * DH * 2) + (qc0 + 8 * hf) * 2, i * (HQ * DH * 2), W_AUX);
    const u32x2 ep = __builtin_amdgcn_raw_buffer_load_b64(rws, WS_EPOCH, 0, 16);
    SB();
    __builtin_amdgcn_s_barrier();
    SB();
    const unsigned xw[7] = {xa[0], xa[1], xa[2], xa[3], xb[0], xb[1], xb[2]};
    f32x2 a[4];
#pragma unroll
    for (int k = 0; k < 4; ++k) a[k] = f32x2{0.f, 0.f};
#pragma unroll
    for (int i = 0; i < 14; ++i) {
      const float xv = (i & 1) ? bfhi(xw[i >> 1]) : bflo(xw[i >> 1]);
#pragma unroll
      for (int k = 0; k < 4; ++k) a[k] = __builtin_elementwise_fma(f32x2{xv, xv}, f32x2{bflo(w[i][k]), bfhi(w[i][k])}, a[k]);
    }
    float v[8] = {a[0][0], a[0][1], a[1][0], a[1][1], a[2][0], a[2][1], a[3][0], a[3][1]};
#pragma unroll
    for (int h = 0; h < 4; ++h) { const float t1 = v[h] + dpp<XOR8>(v[h]), t2 = v[h + 4] + dpp<XOR8>(v[h + 4]); v[h] = (lane & 8) ? t2 : t1; }
#pragma unroll
    for (int h = 0; h < 2; ++h) { const float t1 = v[h] + dpp<ROR12>(v[h]), t2 = v[h + 2] + dpp<ROR4>(v[h + 2]); v[h] = (lane & 4) ? t2 : t1; }
    { const float t1 = v[0] + dpp<XOR2>(v[0]), t2 = v[1] + dpp<XOR2>(v[1]); v[0] = (lane & 2) ? t2 : t1; }
    const float qv = sw32_add(sw16_add(v[0]));
    const unsigned long long tg = (((unsigned long long)ep[1] << 32) | ep[0]) + 1ull;
    const unsigned tlo = (unsigned)tg << 16, thi = (unsigned)(tg >> 16);
    if (lane < 16 && !(X_NOQ & 2)) {
      const int e = ((lane >> 3) & 1) * 4 + ((lane >> 2) & 1) * 2 + ((lane >> 1) & 1);
      __builtin_amdgcn_raw_buffer_store_b64(u32x2{tlo | f2bf(qv), thi}, rws, WS_QG + (qc0 + 8 * hf + e) * 8, 0, 16);
    }
    STAMP(1);
    {
      u32x4 gq[4]; unsigned spins = 0;
      unsigned go = WS_QG + (g * 512 + 8 * lane) * 8;
      u32x4 pk = {0u, 0u, 0u, 0u};
      while ((unsigned)now() - tc0 < QPOLL_T) __builtin_amdgcn_s_sleep(2);
      for (; !(X_NOQ & 2);) {
        CBAR();
        bool ok = true;
#pragma unroll
        for (int i = 0; i < 4; ++i) gq[i] = __builtin_amdgcn_raw_buffer_load_b128(rws, go, i * 16, 16);
#pragma unroll
        for (int i = 0; i < 4; ++i) ok &= gq[i][1] == thi && gq[i][3] == thi && (gq[i][0] & 0xffff0000u) == tlo && (gq[i][2] & 0xffff0000u) == tlo;
        if (ok) {
#pragma unroll
          for (int i = 0; i < 4; ++i) pk[i] = (gq[i][0] & 0xffffu) | (gq[i][2] << 16);
          go = 0x80000000u;
        }
        if (__all(go == 0x80000000u) || ++spins > SPIN_CAP) break;
        if (QSLEEP) __builtin_amdgcn_s_sleep(QSLEEP);
      }
      *reinterpret_cast<u32x4*>(&lds[L_Q + 4 * lane]) = pk;
      if (lane == 0) { lds[L_TAG] = 2u * (unsigned)tg + 1u; lds[L_TAG + 1] = (unsigned)tg; lds[L_TAG + 2] = (unsigned)(tg >> 32); }
      LGKM0();
      lds_poke(lbase + L_QRDY * 4, 1u);
    }
    STAMP(15);
    WO_LOAD();
  }
  __syncthreads();
  if (wave == 0) STAMP(4);
  if (X_NOTAIL) { if (STAMPS && wave == 1 && lane == 0) { unsigned long long* sp = reinterpret_cast<unsigned long long*>(ws + WS_STAMP) + b * 32; sp[9] = (unsigned)__builtin_amdgcn_s_getreg(20 | (3 << 11)) & 0xfu; sp[11] = 0; } if (wof[3][1][0] == 123.456f) out[1] = 1; return; }
  if (STAMPS && wave == 1 && lane == 0) { unsigned long long* sp = reinterpret_cast<unsigned long long*>(ws + WS_STAMP) + b * 32; sp[9] = (unsigned)__builtin_amdgcn_s_getreg(20 | (3 << 11)) & 0xfu; sp[11] = lds[L_FIN + 1]; }
  {
    const float* yl = reinterpret_cast<const float*>(&lds[L_Y2]);
    const f32x4 ya = *reinterpret_cast<const f32x4*>(yl + 224 * wave + 4 * lane);
    const float gate = yl[512];
    const unsigned tag = lds[L_TAG], r8 = 8 * (lane & 3);
    u32x4 pa, pb, ma, mb; unsigned spins = 0;
    const unsigned pbase = WS_P1 + (g * NJ + j) * NJ * 64 + lane * 16, mbase = WS_ML + ((g * 2 + (j >> 4)) * NJ + (lane >> 2)) * 16;
    for (;;) {
      CBAR();
      pa = __builtin_amdgcn_raw_buffer_load_b128(rws, pbase, 0, 16);
      pb = __builtin_amdgcn_raw_buffer_load_b128(rws, pbase + 1024, 0, 16);
      ma = __builtin_amdgcn_raw_buffer_load_b128(rws, mbase, 0, 16);
      mb = __builtin_amdgcn_raw_buffer_load_b128(rws, mbase + 256, 0, 16);
      const bool ok = (((rec_fold(pa, r8) ^ tag) | (rec_fold(pb, r8) ^ tag)) | ((ma[0] ^ rotl(ma[1], 16) ^ ma[2] ^ tag) | (mb[0] ^ rotl(mb[1], 16) ^ mb[2] ^ tag))) == 0u;
      if (__all(ok) || ++spins > SPIN_CAP) break;
      if (PSLEEP) __builtin_amdgcn_s_sleep(PSLEEP);
    }
    if (wave == 0) STAMP(5);
    const float m_a = __uint_as_float(ma[0]), m_b = __uint_as_float(mb[0]);
    float M = fmaxf(m_a, m_b);
    M = fmaxf(M, dpp<ROR4>(M)); M = fmaxf(M, dpp<XOR8>(M)); M = sw32_max(sw16_max(M));
    const float wa = __builtin_amdgcn_exp2f(m_a - M), wb = __builtin_amdgcn_exp2f(m_b - M);
    const float L = red_q(wa * __uint_as_float(ma[1]) + wb * __uint_as_float(mb[1]));
    const float sc = gate / L;
    float o[4];
#pragma unroll
    for (int k = 0; k < 4; ++k) o[k] = red_q(wa * rec_val(pa[k]) + wb * rec_val(pb[k]));
    f32x2 y[2] = {f32x2{0.f, 0.f}, f32x2{0.f, 0.f}};
#define YROW(qd, k) { const float ov = dpp<(qd) * 0x55>(o[k]); _Pragma("unroll") for (int k2 = 0; k2 < 2; ++k2) \
      y[k2] = __builtin_elementwise_fma(f32x2{ov, ov}, wof[4 * (qd) + (k)][k2], y[k2]); }
#define YQ(qd) YROW(qd, 0) YROW(qd, 1) YROW(qd, 2) YROW(qd, 3)
    YQ(0) YQ(1) YQ(2) YQ(3)
    const u32x4 yv = rec_seal(f32x4{fmaf(sc, y[0][0], ya[0]), fmaf(sc, y[0][1], ya[1]), fmaf(sc, y[1][0], ya[2]), fmaf(sc, y[1][1], ya[3])}, tag, r8);
    if (lane < 56)
      __builtin_amdgcn_raw_buffer_store_b128(yv, rws, WS_Y + (((wave * 14 + (lane >> 2)) * NWG + b) * 64 + (lane & 3) * 16), 0, 16);
    if (wave == 0) STAMP(6);
  }
}

extern "C" void kernel_launch(void* const* d_in, const int* in_sizes, int n_in, void* d_out, int out_size, void* d_ws, size_t ws_size, hipStream_t stream) {
  k_fused<<<NWG, NTHR, 0, stream>>>((const unsigned short*)d_in[0], (const unsigned char*)d_in[1], (const unsigned char*)d_in[2], (const unsigned short*)d_in[3],
                                    (const unsigned short*)d_in[4], (const unsigned short*)d_in[6], (const unsigned short*)d_in[7], (const unsigned short*)d_in[8],
                                    (const unsigned short*)d_in[9], (const unsigned short*)d_in[10], (unsigned short*)d_out, (unsigned char*)d_ws);
}
```

```cpp
#include <hip/hip_runtime.h>
#include <hip/hip_bf16.h>
#include <cstdint>

#define NC 4
#define HD 5
#define RT 16
#define GL 2
#define WIN 22
#define LAZY 12
#define LAZYN 16
#define PIPE 5
#define HPOLL_T 1500
#define QPOLL_T 400

constexpr int DM = 448, HQ = 16, HKV = 8, SK = 65536, DH = 256, FF = 1792;
constexpr int NWG = 256, NJ = 32, NWAVE = NC + 3, NTHR = NWAVE * 64;
constexpr int TILE_BYTES = 4096;
constexpr int RANGE_TILES = SK / 8 / NJ;
static_assert(NC >= 4 && NC <= 8 && RT % 4 == 0 && RANGE_TILES % NC == 0 && HD >= 1 && HD <= 6 && (GL == 2 || GL == 4) && WIN % GL == 0 && WIN <= 40, "deal");
constexpr int NRED = 28;
constexpr unsigned SPIN_CAP = 1u << 20, OOB = 0x80000000u;

constexpr unsigned WS_EPOCH = 0, WS_QG = 8192, WS_HP = WS_QG + 4 * 4096 * 4, WS_ML = WS_HP + 8 * FF * 4, WS_P1 = WS_ML + HQ * NJ * 16,
                   WS_Y = WS_P1 + HKV * NJ * NJ * 64, WS_END = WS_Y + NRED * NWG * 64;
constexpr int L_Q = RT * 2048, L_FULL = L_Q + 256, L_QRDY = L_FULL + 1, L_FIN = L_FULL + 2, L_YRDY = L_FULL + 3, L_FREE = L_FULL + 4, L_CH = L_FULL + 12,
              L_H1 = L_FULL + 20, L_TAG = L_FULL + 24, L_FINV = L_FULL + 28, L_Y2 = L_FINV + 64, L_REC = L_Y2 + 520, REC = 516,
              L_END = L_REC + (NC - 1) * REC;
static_assert(L_END * 4 <= 160 * 1024, "LDS");

using bf16x2 = __attribute__((ext_vector_type(2))) __bf16;
using u32x2  = __attribute__((ext_vector_type(2))) unsigned;
using u32x3  = __attribute__((ext_vector_type(3))) unsigned;
using u32x4  = __attribute__((ext_vector_type(4))) unsigned;
using f32x2  = __attribute__((ext_vector_type(2))) float;
using f32x4  = __attribute__((ext_vector_type(4))) float;
#define LAS __attribute__((address_space(3)))
#define RLX_WG __ATOMIC_RELAXED, __HIP_MEMORY_SCOPE_WORKGROUP
#define CBAR() asm volatile("" ::: "memory")
#define SB() __builtin_amdgcn_sched_barrier(0)
#define LGKM0() asm volatile("s_waitcnt lgkmcnt(0)" ::: "memory")
#define STR2(x) #x
#define STR(x) STR2(x)
#define RSRC(p, bytes) __builtin_amdgcn_make_buffer_rsrc((void*)(p), 0, (int)(bytes), 0x00020000)
#define LD128(r, off, soff, aux) __builtin_amdgcn_raw_buffer_load_b128(r, off, soff, aux)
#define ST128(v, r, off) __builtin_amdgcn_raw_buffer_store_b128(v, r, off, 0, 16)

__device__ __forceinline__ float dot2(unsigned k, unsigned q, float acc) {
  return __builtin_amdgcn_fdot2_f32_bf16(__builtin_bit_cast(bf16x2, k), __builtin_bit_cast(bf16x2, q), acc, false);
}
template <int CTRL> __device__ __forceinline__ float dpp(float x) {
  return __builtin_bit_cast(float, __builtin_amdgcn_mov_dpp(__builtin_bit_cast(int, x), CTRL, 0xf, 0xf, true));
}
template <int CTRL> __device__ __forceinline__ unsigned dppu(unsigned x) { return (unsigned)__builtin_amdgcn_mov_dpp((int)x, CTRL, 0xf, 0xf, true); }
constexpr int XOR1 = 0xB1, XOR2 = 0x4E, XOR7 = 0x141, XOR8 = 0x128, ROR4 = 0x124;
__device__ __forceinline__ float bflo(unsigned u) { return __builtin_bit_cast(float, u << 16); }
__device__ __forceinline__ float bfhi(unsigned u) { return __builtin_bit_cast(float, u & 0xffff0000u); }
__device__ __forceinline__ unsigned f2bf(float f) { __hip_bfloat16 b = __float2bfloat16(f); return __builtin_bit_cast(unsigned short, b); }
__device__ __forceinline__ float tr16(float a, float b) { auto s = __builtin_amdgcn_permlane16_swap(__float_as_uint(a), __float_as_uint(b), false, false); return __uint_as_float(s[0]) + __uint_as_float(s[1]); }
__device__ __forceinline__ float tr32(float a, float b) { auto s = __builtin_amdgcn_permlane32_swap(__float_as_uint(a), __float_as_uint(b), false, false); return __uint_as_float(s[0]) + __uint_as_float(s[1]); }
__device__ __forceinline__ float sw16_add(float x) { return tr16(x, x); }
__device__ __forceinline__ float sw32_add(float x) { return tr32(x, x); }
__device__ __forceinline__ float sw16_max(float x) { auto s = __builtin_amdgcn_permlane16_swap(__float_as_uint(x), __float_as_uint(x), false, false); return fmaxf(__uint_as_float(s[0]), __uint_as_float(s[1])); }
__device__ __forceinline__ float sw32_max(float x) { auto s = __builtin_amdgcn_permlane32_swap(__float_as_uint(x), __float_as_uint(x), false, false); return fmaxf(__uint_as_float(s[0]), __uint_as_float(s[1])); }
__device__ __forceinline__ float red_q(float x) { x += dpp<ROR4>(x); x += dpp<XOR8>(x); return sw32_add(sw16_add(x)); }
__device__ __forceinline__ float red_all(float x) { x += dpp<XOR1>(x); x += dpp<XOR2>(x); return red_q(x); }
__device__ __forceinline__ float red4(float v0, float v1, float v2, float v3) {
  float u = tr32(tr16(v0, v1), tr16(v2, v3));
  u += dpp<ROR4>(u); u += dpp<XOR8>(u);
  return u;
}
__device__ __forceinline__ unsigned rotl(unsigned x, unsigned r) { return __builtin_amdgcn_alignbit(x, x, (32u - r) & 31u); }
__device__ __forceinline__ unsigned rec_fold(u32x4 w, unsigned r8) {
  unsigned h = rotl(w[0] ^ rotl(w[1], 2) ^ rotl(w[2], 4) ^ rotl(w[3], 6), r8);
  h ^= dppu<XOR1>(h); h ^= dppu<XOR2>(h);
  return h;
}
__device__ __forceinline__ u32x4 rec_seal(f32x4 v, unsigned tag, unsigned r8) {
  u32x4 w = {__float_as_uint(v[0]) & ~3u, __float_as_uint(v[1]) & ~3u, __float_as_uint(v[2]) & ~3u, __float_as_uint(v[3]) & ~3u};
  const unsigned cb = (rec_fold(w, r8) ^ tag) >> r8;
  w[0] |= cb & 3u; w[1] |= (cb >> 2) & 3u; w[2] |= (cb >> 4) & 3u; w[3] |= (cb >> 6) & 3u;
  return w;
}
__device__ __forceinline__ float rec_val(unsigned w) { return __uint_as_float(w & ~3u); }
__device__ __forceinline__ f32x4 proj_reduce(const f32x2 (&a)[4], int lane) {
  f32x4 r;
#pragma unroll
  for (int k = 0; k < 4; ++k) {
    const float lo = a[k >> 1][k & 1], hi = a[2 + (k >> 1)][k & 1];
    const float t1 = lo + dpp<XOR1>(lo), t2 = hi + dpp<XOR1>(hi);
    r[k] = sw32_add(sw16_add((lane & 1) ? t2 : t1));
  }
  return r;
}
__device__ __forceinline__ unsigned now() { return (unsigned)__builtin_amdgcn_s_memrealtime(); }
__device__ __forceinline__ unsigned lds_peek(unsigned byte_addr) {
  unsigned v;
  asm volatile("ds_read_b32 %0, %1\n\ts_waitcnt lgkmcnt(0)" : "=v"(v) : "v"(byte_addr) : "memory");
  return __builtin_amdgcn_readfirstlane(v);
}
__device__ __forceinline__ void lds_poke(unsigned byte_addr, unsigned v) { asm volatile("ds_write_b32 %0, %1" :: "v"(byte_addr), "v"(v) : "memory"); }
__device__ __forceinline__ void lds_wait(unsigned byte_addr, unsigned atleast) {
  for (unsigned spins = 0; lds_peek(byte_addr) < atleast && spins < SPIN_CAP; ++spins) __builtin_amdgcn_s_sleep(1);
}

__global__ void __launch_bounds__(NTHR, (NWAVE + 3) / 4)
k_fused(const unsigned char* __restrict__ Kc, const unsigned char* __restrict__ Vc, const unsigned short* __restrict__ x,
        const unsigned short* __restrict__ wq, const unsigned short* __restrict__ wo, const unsigned short* __restrict__ w1,
        const unsigned short* __restrict__ b1, const unsigned short* __restrict__ w2, const unsigned short* __restrict__ b2,
        const unsigned short* __restrict__ gw, unsigned short* __restrict__ out, unsigned char* ws) {
  __shared__ __attribute__((aligned(16))) unsigned lds[L_END];
  const unsigned lbase = (unsigned)(size_t)(LAS unsigned*)lds;
  const int b = blockIdx.x;
  const int tid = threadIdx.x, lane = tid & 63, wave = __builtin_amdgcn_readfirstlane(tid >> 6);

  if (__builtin_expect(wave == 0, 1)) {
    __builtin_amdgcn_s_setprio(3);
    const auto rk = RSRC(Kc, HKV * SK * DH * 2);
    const auto rv = RSRC(Vc, HKV * SK * DH * 2);
    const unsigned voff = lane * 16;
    unsigned off = (unsigned)b * (unsigned)(RANGE_TILES * TILE_BYTES), slot = 0;
    const unsigned fulla = lbase + L_FULL * 4, ctla = lbase + (L_FREE + (lane < NC ? lane : 0)) * 4;
    int issued = 0;
#define DMA(R, S, O) __builtin_amdgcn_raw_ptr_buffer_load_lds(R, (LAS void*)((LAS unsigned char*)lds + (S)), 16, voff, off, O, 2)
#define PUB() do { asm volatile("s_waitcnt vmcnt(" STR(WIN - GL) ")" ::: "memory"); lds_poke(fulla, max(lmax, issued - (WIN - GL))); issued += GL; } while (0)
#define PART(R, S) do { PUB(); DMA(R, S, 0); DMA(R, S, 1024); if (GL == 2) PUB(); DMA(R, S, 2048); DMA(R, S, 3072); } while (0)
#define TILE() do { PART(rk, slot); PART(rv, slot + TILE_BYTES); off += TILE_BYTES; slot += 2 * TILE_BYTES; } while (0)
    int lmax = 0;
    DMA(rk, 0, 0); DMA(rk, 0, 1024); DMA(rk, 0, 2048); DMA(rk, 0, 3072);
    DMA(rv, TILE_BYTES, 0); DMA(rv, TILE_BYTES, 1024); DMA(rv, TILE_BYTES, 2048); DMA(rv, TILE_BYTES, 3072);
    off += TILE_BYTES; slot = 2 * TILE_BYTES; issued = 8;
    if (lane < 24) lds_poke(fulla + lane * 4, 0u);
    TILE();
    LGKM0();
    __builtin_amdgcn_s_barrier();
    TILE(); TILE();
    for (int t = 4; t < RANGE_TILES; t += 4) {
      if (slot == RT * 2 * TILE_BYTES) slot = 0;
      const unsigned need = t + 4 > RT ? (unsigned)(t + 4 - RT + NC - 1) / NC : 0u;
      unsigned v;
      asm volatile("ds_read_b32 %0, %1\n\ts_waitcnt lgkmcnt(0)" : "=v"(v) : "v"(ctla) : "memory");
      if (__any(v < need)) {
        asm volatile("s_waitcnt vmcnt(0)" ::: "memory");
        lmax = issued; lds_poke(fulla, issued);
        unsigned spins = 0;
        do { __builtin_amdgcn_s_sleep(1); asm volatile("ds_read_b32 %0, %1\n\ts_waitcnt lgkmcnt(0)" : "=v"(v) : "v"(ctla) : "memory"); } while (__any(v < need) && ++spins < SPIN_CAP);
      }
      TILE(); TILE(); TILE(); TILE();
    }
#define TAILPUB(n) if (WIN > (n)) { asm volatile("s_waitcnt vmcnt(" #n ")" ::: "memory"); lds_poke(fulla, RANGE_TILES * 8 - (n)); }
    TAILPUB(36) TAILPUB(32) TAILPUB(28) TAILPUB(24) TAILPUB(20) TAILPUB(16) TAILPUB(12) TAILPUB(8) TAILPUB(4) TAILPUB(0)
    return;
  }

  const int g = b >> 5, j = b & 31;
  const unsigned r8 = 8 * (lane & 3);
  const auto rws = RSRC(ws, WS_END);

  if (wave >= 3) {
    const int sw = wave - 3, c = lane & 31, t = lane & 15;
    constexpr float C2 = 0.0625f * 1.4426950408889634f;
    __builtin_amdgcn_s_barrier();
    u32x4 q0, q1;
    float m = -INFINITY, l = 0.f;
    f32x2 acc0[4], acc1[4];
#pragma unroll
    for (int k = 0; k < 4; ++k) { acc0[k] = f32x2{0.f, 0.f}; acc1[k] = f32x2{0.f, 0.f}; }
    float x_[8], p;
    unsigned full = 0, tag;
#define WAITFULL(n) do { for (unsigned spins = 0; full < (unsigned)(n) && spins < SPIN_CAP; ++spins) { full = lds_peek(lbase + L_FULL * 4); if (full >= (unsigned)(n)) break; __builtin_amdgcn_s_sleep(1); } } while (0)
#define RD4(dst, tile, part) do { const u32x4* src_ = reinterpret_cast<const u32x4*>(&lds[(((tile) % RT) * 2 + (part)) * 1024 + lane * 4]); \
      CBAR(); dst[0] = src_[0]; dst[1] = src_[64]; dst[2] = src_[128]; dst[3] = src_[192]; } while (0)
#define PIN4(a) asm volatile("" : "+v"(a[0]), "+v"(a[1]), "+v"(a[2]), "+v"(a[3]) :: "memory")
#define RELEASE(n) do { LGKM0(); lds_poke(lbase + (L_FREE + sw) * 4, (n)); } while (0)
#define QK1(KK, i) { float a_ = 0.f, b_ = 0.f; \
      _Pragma("unroll") for (int k = 0; k < 4; ++k) { a_ = dot2((KK)[k], q0[k], a_); b_ = dot2((KK)[k], q1[k], b_); } \
      x_[2 * (i)] = a_; x_[2 * (i) + 1] = b_; }
#define TR_STEP(HALF, CTRL, BIT) _Pragma("unroll") for (int h = 0; h < (HALF); ++h) { \
      const float lo_ = x_[h], hi_ = x_[h + (HALF)]; const float t1_ = lo_ + dpp<CTRL>(lo_), t2_ = hi_ + dpp<CTRL>(hi_); \
      x_[h] = (t & (BIT)) ? t2_ : t1_; }
#define SOFTMAX() { TR_STEP(4, XOR8, 8) TR_STEP(2, XOR7, 4) TR_STEP(1, XOR2, 2) x_[0] += dpp<XOR1>(x_[0]); \
      const float sc = sw16_add(x_[0]) * C2; \
      if (__any(sc > m)) {                                       \
        float pm = sc; pm = fmaxf(pm, dpp<ROR4>(pm)); pm = fmaxf(pm, dpp<XOR8>(pm)); pm = sw32_max(pm); \
        const float mn = fmaxf(m, pm), alpha = __builtin_amdgcn_exp2f(m - mn); \
        m = mn; l *= alpha; \
        const float a0 = dpp<0x00>(alpha), a1 = dpp<0xAA>(alpha); \
        _Pragma("unroll") for (int k = 0; k < 4; ++k) { acc0[k] *= a0; acc1[k] *= a1; } } \
      p = __builtin_amdgcn_exp2f(sc - m); l += p; asm volatile("" : "+v"(p)); }
#define PV1(VV, i) { const float p0 = dpp<0x150 + 4 * (i)>(p), p1 = dpp<0x150 + 4 * (i) + 2>(p); \
      _Pragma("unroll") for (int k = 0; k < 4; ++k) { const f32x2 vf = {bflo((VV)[k]), bfhi((VV)[k])}; \
        acc0[k] = __builtin_elementwise_fma(f32x2{p0, p0}, vf, acc0[k]); acc1[k] = __builtin_elementwise_fma(f32x2{p1, p1}, vf, acc1[k]); } }
#define PINACC() _Pragma("unroll") for (int k = 0; k < 4; ++k) { asm volatile("" : "+v"(acc0[k])); asm volatile("" : "+v"(acc1[k])); }
    {
      {
        {
          u32x4 Kb[HD][4], Vb[HD][4];
#pragma unroll
          for (int d = 0; d < HD; ++d) {
            const int tile = d * NC + sw;
            WAITFULL(tile * 8 + 8);
            RD4(Kb[d], tile, 0); RD4(Vb[d], tile, 1);
            RELEASE(d + 1);
            PIN4(Kb[d]); PIN4(Vb[d]);
          }
          lds_wait(lbase + L_QRDY * 4, 1u);
          q0 = *reinterpret_cast<const u32x4*>(&lds[L_Q + 4 * c]); q1 = *reinterpret_cast<const u32x4*>(&lds[L_Q + 128 + 4 * c]);
          tag = lds_peek(lbase + L_TAG * 4);
#pragma unroll
          for (int d = 0; d < HD; ++d) {
            QK1(Kb[d][0], 0) QK1(Kb[d][1], 1) QK1(Kb[d][2], 2) QK1(Kb[d][3], 3)
            SOFTMAX();
            PV1(Vb[d][0], 0) PV1(Vb[d][1], 1) PV1(Vb[d][2], 2) PV1(Vb[d][3], 3)
            PINACC(); SB();
          }
        }
        for (int n = HD; n < RANGE_TILES / NC; ++n) {
          const int tile = n * NC + sw;
          u32x4 Kb[4], Vb[4];
          WAITFULL(tile * 8 + 4);
          RD4(Kb, tile, 0);
          QK1(Kb[0], 0) QK1(Kb[1], 1) QK1(Kb[2], 2) QK1(Kb[3], 3)
          SOFTMAX();
          WAITFULL(tile * 8 + 8);
          RD4(Vb, tile, 1);
          RELEASE(n + 1);
          PIN4(Vb);
          PV1(Vb[0], 0) PV1(Vb[1], 1) PV1(Vb[2], 2) PV1(Vb[3], 3)
          PINACC(); SB();
        }
      }
      f32x4 ra, rb;
#pragma unroll
      for (int k = 0; k < 2; ++k)
#pragma unroll
        for (int e = 0; e < 2; ++e) { ra[2 * k + e] = tr32(acc0[k][e], acc1[k][e]); rb[2 * k + e] = tr32(acc0[2 + k][e], acc1[2 + k][e]); }
      float lt = l; lt += dpp<ROR4>(lt); lt += dpp<XOR8>(lt); lt = sw32_add(lt);
      float mh = lane < 32 ? dpp<0x00>(m) : dpp<0xAA>(m), lh = lane < 32 ? dpp<0x00>(lt) : dpp<0xAA>(lt);
      if (sw == NC - 1) {
        for (unsigned spins = 0; spins < SPIN_CAP; ++spins) {
          unsigned fl;
          asm volatile("ds_read_b32 %0, %1\n\ts_waitcnt lgkmcnt(0)" : "=v"(fl) : "v"(lbase + (L_CH + (lane < NC - 1 ? lane : 0)) * 4) : "memory");
          if (__all(fl != 0u)) break;
        }
        f32x2 ml[NC - 1]; f32x4 pa[NC - 1], pb[NC - 1];
        float M = mh;
#pragma unroll
        for (int w = 0; w < NC - 1; ++w) {
          const float* pr = reinterpret_cast<const float*>(&lds[L_REC + w * REC]);
          ml[w] = *reinterpret_cast<const f32x2*>(pr + 2 * (lane >> 5));
          pa[w] = *reinterpret_cast<const f32x4*>(pr + 4 + 8 * lane); pb[w] = *reinterpret_cast<const f32x4*>(pr + 8 + 8 * lane);
        }
#pragma unroll
        for (int w = 0; w < NC - 1; ++w) M = fmaxf(M, ml[w][0]);
        const float wa = __builtin_amdgcn_exp2f(mh - M);
        ra *= wa; rb *= wa; lh *= wa; mh = M;
#pragma unroll
        for (int w = 0; w < NC - 1; ++w) {
          const float wp = __builtin_amdgcn_exp2f(ml[w][0] - M);
          ra += wp * pa[w]; rb += wp * pb[w]; lh += wp * ml[w][1];
        }
      }
      if (sw < NC - 1) {
        float* rec = reinterpret_cast<float*>(&lds[L_REC + sw * REC]);
        *reinterpret_cast<f32x4*>(rec + 4 + 8 * lane) = ra;
        *reinterpret_cast<f32x4*>(rec + 8 + 8 * lane) = rb;
        if (c == 0) *reinterpret_cast<f32x2*>(rec + 2 * (lane >> 5)) = f32x2{mh, lh};
        LGKM0();
        lds_poke(lbase + (L_CH + sw) * 4, 1u);
      } else {
        const unsigned po = WS_P1 + ((g * NJ + 2 * (lane >> 2)) * NJ + j) * 64 + (lane & 3) * 16;
        ST128(rec_seal(ra, tag, r8), rws, po);
        ST128(rec_seal(rb, tag, r8), rws, po + NJ * 64);
        if (c == 0) ST128((u32x4{__float_as_uint(mh), __float_as_uint(lh), __float_as_uint(mh) ^ rotl(__float_as_uint(lh), 16) ^ tag, 0u}), rws, WS_ML + ((g * 2 + (lane >> 5)) * NJ + j) * 16);
      }
      if (b < NRED && sw < 4) {
        u32x4 v[4], w_[4]; unsigned spins = 0u;
        const unsigned ybase = WS_Y + (b * NWG + sw * 64) * 64 + lane * 16;
        lds_wait(lbase + L_H1 * 4, 1u);
#define YLOAD(dst) do { CBAR(); _Pragma("unroll") for (int i = 0; i < 4; ++i) dst[i] = LD128(rws, ybase + i * 1024, 0, 16); CBAR(); } while (0)
#define YOK(src) __all((((rec_fold(src[0], r8) ^ tag) | (rec_fold(src[1], r8) ^ tag)) | ((rec_fold(src[2], r8) ^ tag) | (rec_fold(src[3], r8) ^ tag))) == 0u)
        YLOAD(v);
        __builtin_amdgcn_s_sleep(PIPE);
        for (;;) {
          YLOAD(w_);
          if (YOK(v)) break;
          YLOAD(v);
          if (YOK(w_) || ++spins > SPIN_CAP) {
#pragma unroll
            for (int i = 0; i < 4; ++i) v[i] = w_[i];
            break;
          }
        }
        float s[4];
#pragma unroll
        for (int k = 0; k < 4; ++k) s[k] = (rec_val(v[0][k]) + rec_val(v[1][k])) + (rec_val(v[2][k]) + rec_val(v[3][k]));
        const float u = red4(s[0], s[1], s[2], s[3]);
        float* fin = reinterpret_cast<float*>(&lds[L_FINV]);
        if ((lane & 12) == 0) fin[sw * 16 + (lane & 3) * 4 + (lane >> 4)] = u;
        unsigned old = 0;
        CBAR();
        if (lane == 0) old = __hip_atomic_fetch_add((LAS unsigned*)&lds[L_FIN], 1u, RLX_WG);
        old = __builtin_amdgcn_readfirstlane(old);
        CBAR();
        if (old == 3u) {
          if (lane < 16) out[b * 16 + lane] = (unsigned short)f2bf((fin[lane] + fin[16 + lane]) + (fin[32 + lane] + fin[48 + lane]));
          if (b == 0 && lane == 0) __builtin_amdgcn_raw_buffer_store_b64(u32x2{lds[L_TAG + 1], lds[L_TAG + 2]}, rws, WS_EPOCH, 0, 16);
        }
      }
    }
    return;
  }

  const int cw = wave - 1;
  const unsigned tc0 = now();
  const int rr = (lane & 1) | ((lane >> 4) << 1);
  unsigned tag;
  if (cw == 0) {
    const int qcg = b >> 2, qrg = b & 3, row0 = qrg * 112 + rr * 14;
    const auto rx = RSRC(x, DM * 2);
    const auto rq = RSRC(wq, DM * HQ * DH * 2);
    u32x4 w[14];
#pragma unroll
    for (int i = 0; i < 14; ++i) w[i] = LD128(rq, row0 * (HQ * DH * 2) + qcg * 128 + (lane & 14) * 8, i * (HQ * DH * 2), 2);
    const u32x4 xa = LD128(rx, row0 * 2, 0, 0);
    const u32x3 xb = __builtin_amdgcn_raw_buffer_load_b96(rx, row0 * 2 + 16, 0, 0);
    const u32x2 ep = __builtin_amdgcn_raw_buffer_load_b64(rws, WS_EPOCH, 0, 16);
    SB();
    __builtin_amdgcn_s_barrier();
    SB();
    const unsigned xw[7] = {xa[0], xa[1], xa[2], xa[3], xb[0], xb[1], xb[2]};
    f32x2 a[4];
#pragma unroll
    for (int k = 0; k < 4; ++k) a[k] = f32x2{0.f, 0.f};
#pragma unroll
    for (int i = 0; i < 14; ++i) {
      const float xv = (i & 1) ? bfhi(xw[i >> 1]) : bflo(xw[i >> 1]);
#pragma unroll
      for (int k = 0; k < 4; ++k) a[k] = __builtin_elementwise_fma(f32x2{xv, xv}, f32x2{bflo(w[i][k]), bfhi(w[i][k])}, a[k]);
    }
    const unsigned long long tg = (((unsigned long long)ep[1] << 32) | ep[0]) + 1ull;
    tag = 2u * (unsigned)tg + 1u;
    {
      const u32x4 rec = rec_seal(proj_reduce(a, lane), tag, r8);
      if (lane < 16) ST128(rec, rws, WS_QG + (qrg * 4096 + qcg * 64) * 4 + lane * 16);
    }
    u32x4 gq[8]; unsigned go[8], spins = 0;
#pragma unroll
    for (int i = 0; i < 8; ++i) { go[i] = WS_QG + ((i >> 1) * 4096 + g * 512 + (i & 1) * 256) * 4 + lane * 16; gq[i] = u32x4{0u, 0u, 0u, 0u}; }
    while (now() - tc0 < QPOLL_T) __builtin_amdgcn_s_sleep(2);
    for (;;) {
      CBAR();
      u32x4 t_[8];
#pragma unroll
      for (int i = 0; i < 8; ++i) t_[i] = LD128(rws, go[i], 0, 16);
      bool all = true;
#pragma unroll
      for (int i = 0; i < 8; ++i) {
        if (rec_fold(t_[i], r8) == tag) { gq[i] = t_[i]; go[i] = OOB; }
        all &= go[i] == OOB;
      }
      if (__all(all) || ++spins > SPIN_CAP) break;
      __builtin_amdgcn_s_sleep(8);
    }
#pragma unroll
    for (int hf = 0; hf < 2; ++hf) {
      float qs[4];
#pragma unroll
      for (int k = 0; k < 4; ++k) qs[k] = (rec_val(gq[hf][k]) + rec_val(gq[2 + hf][k])) + (rec_val(gq[4 + hf][k]) + rec_val(gq[6 + hf][k]));
      *reinterpret_cast<u32x2*>(&lds[L_Q + hf * 128 + 2 * lane]) = u32x2{f2bf(qs[0]) | (f2bf(qs[1]) << 16), f2bf(qs[2]) | (f2bf(qs[3]) << 16)};
    }
    if (lane == 0) { lds[L_TAG] = tag; lds[L_TAG + 1] = (unsigned)tg; lds[L_TAG + 2] = (unsigned)(tg >> 32); }
    LGKM0();
    lds_poke(lbase + L_QRDY * 4, 1u);
  } else {
    __builtin_amdgcn_s_barrier();
    lds_wait(lbase + L_QRDY * 4, 1u);
    tag = lds_peek(lbase + L_TAG * 4);
    const bool mlp = b < 224;
    const auto rx = RSRC(x, DM * 2);
    const auto rg = RSRC(gw, DM * 2);
    const auto rb2 = RSRC(b2, DM * 2);
    const auto r1 = RSRC(w1, mlp ? DM * FF * 2 : 0);
    const auto r2 = RSRC(w2, mlp ? FF * DM * 2 : 0);
    const auto rb = RSRC(b1, FF * 2);
    {
      const int row0 = (b & 7) * 56 + rr * 7;
      u32x4 w1v[7]; unsigned short xr[7];
#pragma unroll
      for (int i = 0; i < 7; ++i) { w1v[i] = LD128(r1, (row0 + i) * (FF * 2) + (b >> 3) * 128 + (lane & 14) * 8, 0, 2); xr[i] = x[row0 + i]; }
      f32x2 hv[4];
#pragma unroll
      for (int k = 0; k < 4; ++k) hv[k] = f32x2{0.f, 0.f};
#pragma unroll
      for (int i = 0; i < 7; ++i) {
        const float xf = bflo(xr[i]);
#pragma unroll
        for (int k = 0; k < 4; ++k) hv[k] = __builtin_elementwise_fma(f32x2{xf, xf}, f32x2{bflo(w1v[i][k]), bfhi(w1v[i][k])}, hv[k]);
      }
      const u32x4 rec = rec_seal(proj_reduce(hv, lane), tag, r8);
      if (mlp && lane < 16) ST128(rec, rws, WS_HP + ((b & 7) * FF + (b >> 3) * 64) * 4 + lane * 16);
    }
    const int hc0 = (mlp ? b : 0) * 8;
    const u32x4 xv = LD128(rx, lane * 16, 0, 0), gv = LD128(rg, lane * 16, 0, 0), b2v = LD128(rb2, lane * 16, 0, 0);
    const u32x4 bv = LD128(rb, hc0 * 2, 0, 0);
    u32x4 wb[8];
#pragma unroll
    for (int i = 0; i < 8; ++i) wb[i] = LD128(r2, (hc0 + i) * (DM * 2) + lane * 16, 0, 2);
    float s = 0.f;
#pragma unroll
    for (int k = 0; k < 4; ++k) { s = fmaf(bflo(xv[k]), bflo(gv[k]), s); s = fmaf(bfhi(xv[k]), bfhi(gv[k]), s); }
    s = red_all(s);
    const float gate = 1.f / (1.f + __expf(-s));
    const float bsc = b == NWG - 1 ? 1.f - gate : 0.f;
    f32x2 y2[4];
#pragma unroll
    for (int k = 0; k < 4; ++k) y2[k] = f32x2{bsc * bflo(b2v[k]), bsc * bfhi(b2v[k])};
    if (mlp) {
      u32x4 hr; unsigned spins = 0;
      while (now() - tc0 < HPOLL_T) __builtin_amdgcn_s_sleep(16);
      for (;;) {
        CBAR();
        hr = LD128(rws, WS_HP + (((lane >> 2) & 7) * FF + (b >> 1) * 16) * 4 + (lane & 3) * 16, 0, 16);
        if (__all(rec_fold(hr, r8) == tag) || ++spins > SPIN_CAP) break;
        __builtin_amdgcn_s_sleep(16);
      }
      float hs[4];
#pragma unroll
      for (int k = 0; k < 4; ++k) { float v_ = rec_val(hr[k]); v_ += dpp<ROR4>(v_); v_ += dpp<XOR8>(v_); hs[k] = sw16_add(v_); }
#pragma unroll
      for (int k = 0; k < 8; ++k) {
        const float h = __uint_as_float(__builtin_amdgcn_readlane(__float_as_uint(hs[k & 3]), (b & 1) * 2 + (k >> 2))) + ((k & 1) ? bfhi(bv[k >> 1]) : bflo(bv[k >> 1]));
        const float u = 0.7978845608028654f * (h + 0.044715f * h * h * h);
        const float ge = 0.5f * h * (1.f + tanhf(u)) * (1.f - gate);
#pragma unroll
        for (int k2 = 0; k2 < 4; ++k2) y2[k2] = __builtin_elementwise_fma(f32x2{ge, ge}, f32x2{bflo(wb[k][k2]), bfhi(wb[k][k2])}, y2[k2]);
      }
    }
    float* yl = reinterpret_cast<float*>(&lds[L_Y2]);
    *reinterpret_cast<f32x4*>(yl + 8 * lane) = f32x4{y2[0][0], y2[0][1], y2[1][0], y2[1][1]};
    *reinterpret_cast<f32x4*>(yl + 8 * lane + 4) = f32x4{y2[2][0], y2[2][1], y2[3][0], y2[3][1]};
    if (lane == 0) yl[512] = gate;
    LGKM0();
    lds_poke(lbase + L_YRDY * 4, 1u);
  }
  f32x2 wof[16][2];
  {
    u32x2 wov[16];
    const auto ro = RSRC(wo, HQ * DH * DM * 2);
    const int d0 = g * 512 + (j >> 1) * 32 + (j & 1) * 4;
#pragma unroll
    for (int r = 0; r < 16; ++r) wov[r] = __builtin_amdgcn_raw_buffer_load_b64(ro, (d0 + (r >> 2) * 8 + (r & 3)) * (DM * 2) + cw * 448 + lane * 8, 0, 2);
#pragma unroll
    for (int r = 0; r < 16; ++r)
#pragma unroll
      for (int k = 0; k < 2; ++k) { wof[r][k] = f32x2{bflo(wov[r][k]), bfhi(wov[r][k])}; asm volatile("" : "+v"(wof[r][k])); }
  }
  lds_wait(lbase + L_YRDY * 4, 1u);
  const float* yl = reinterpret_cast<const float*>(&lds[L_Y2]);
  const f32x4 ya = *reinterpret_cast<const f32x4*>(yl + 224 * cw + 4 * lane);
  const float gate = yl[512];
  {
    for (unsigned spins = 0; lds_peek(lbase + L_FULL * 4) < (unsigned)(RANGE_TILES * 8) && spins < SPIN_CAP; ++spins) __builtin_amdgcn_s_sleep(4);
    u32x4 pa, pb, ma, mb, pa2, pb2, ma2, mb2; unsigned spins = 0u;
    const unsigned pbase = WS_P1 + (g * NJ + j) * NJ * 64 + lane * 16, mbase = WS_ML + ((g * 2 + (j >> 4)) * NJ + (lane >> 2)) * 16;
#define HLOAD(A, B, C, D) do { CBAR(); A = LD128(rws, pbase, 0, 16); B = LD128(rws, pbase + 1024, 0, 16); C = LD128(rws, mbase, 0, 16); D = LD128(rws, mbase + 256, 0, 16); CBAR(); } while (0)
#define HBAD(A, B, C, D) __builtin_amdgcn_ballot_w64((((rec_fold(A, r8) ^ tag) | (rec_fold(B, r8) ^ tag)) | ((C[0] ^ rotl(C[1], 16) ^ C[2] ^ tag) | (D[0] ^ rotl(D[1], 16) ^ D[2] ^ tag))) != 0u)
    unsigned long long bad;
    for (;;) {
      HLOAD(pa, pb, ma, mb);
      bad = HBAD(pa, pb, ma, mb);
      if (__builtin_popcountll(bad) <= LAZYN || ++spins > SPIN_CAP) break;
      __builtin_amdgcn_s_sleep(LAZY);
    }
    if (bad) {
      HLOAD(pa, pb, ma, mb);
      __builtin_amdgcn_s_sleep(PIPE);
      for (;;) {
        HLOAD(pa2, pb2, ma2, mb2);
        if (!HBAD(pa, pb, ma, mb)) break;
        HLOAD(pa, pb, ma, mb);
        if (!HBAD(pa2, pb2, ma2, mb2) || ++spins > SPIN_CAP) { pa = pa2; pb = pb2; ma = ma2; mb = mb2; break; }
      }
    }
    const float m_a = __uint_as_float(ma[0]), m_b = __uint_as_float(mb[0]);
    float M = fmaxf(m_a, m_b);
    M = fmaxf(M, dpp<ROR4>(M)); M = fmaxf(M, dpp<XOR8>(M)); M = sw32_max(sw16_max(M));
    const float wa = __builtin_amdgcn_exp2f(m_a - M), wb = __builtin_amdgcn_exp2f(m_b - M);
    const float L = red_q(wa * __uint_as_float(ma[1]) + wb * __uint_as_float(mb[1]));
    const float sc = gate * __builtin_amdgcn_rcpf(L);
    const float u = red4(wa * rec_val(pa[0]) + wb * rec_val(pb[0]), wa * rec_val(pa[1]) + wb * rec_val(pb[1]),
                         wa * rec_val(pa[2]) + wb * rec_val(pb[2]), wa * rec_val(pa[3]) + wb * rec_val(pb[3]));
    f32x2 y[2], z[2];
#define OROW(r) __uint_as_float(__builtin_amdgcn_readlane(__float_as_uint(u), 16 * ((r) & 3) + ((r) >> 2)))
    { const float o0 = OROW(0), o1 = OROW(1); y[0] = o0 * wof[0][0]; y[1] = o0 * wof[0][1]; z[0] = o1 * wof[1][0]; z[1] = o1 * wof[1][1]; }
#define YR2(r) { const float o0 = OROW(r), o1 = OROW((r) + 1); \
      y[0] = __builtin_elementwise_fma(f32x2{o0, o0}, wof[r][0], y[0]); y[1] = __builtin_elementwise_fma(f32x2{o0, o0}, wof[r][1], y[1]); \
      z[0] = __builtin_elementwise_fma(f32x2{o1, o1}, wof[(r) + 1][0], z[0]); z[1] = __builtin_elementwise_fma(f32x2{o1, o1}, wof[(r) + 1][1], z[1]); }
    YR2(2) YR2(4) YR2(6) YR2(8) YR2(10) YR2(12) YR2(14)
    y[0] += z[0]; y[1] += z[1];
    const u32x4 yv = rec_seal(f32x4{fmaf(sc, y[0][0], ya[0]), fmaf(sc, y[0][1], ya[1]), fmaf(sc, y[1][0], ya[2]), fmaf(sc, y[1][1], ya[3])}, tag, r8);
    if (lane < 56) ST128(yv, rws, WS_Y + (((cw * 14 + (lane >> 2)) * NWG + b) * 64 + (lane & 3) * 16));
    if (cw == 0) { lds_poke(lbase + L_H1 * 4, 1u); }
  }
}

extern "C" void kernel_launch(void* const* d_in, const int* in_sizes, int n_in, void* d_out, int out_size, void* d_ws, size_t ws_size, hipStream_t stream) {
  k_fused<<<NWG, NTHR, 0, stream>>>((const unsigned char*)d_in[1], (const unsigned char*)d_in[2], (const unsigned short*)d_in[0], (const unsigned short*)d_in[3],
                                    (const unsigned short*)d_in[4], (const unsigned short*)d_in[6], (const unsigned short*)d_in[7], (const unsigned short*)d_in[8],
                                    (const unsigned short*)d_in[9], (const unsigned short*)d_in[10], (unsigned short*)d_out, (unsigned char*)d_ws);
}
```

```cpp
#include <hip/hip_runtime.h>
#include <hip/hip_bf16.h>
#include <cstdint>

#define NC 4
#define HD 10
#define RT 32
#define GL 2
#define WIN 22
#define LAZY 12
#define LAZYN 16
#define PIPE 5
#define HPOLL_T 1500
#define QPOLL_T 400

constexpr int DM = 448, HQ = 16, HKV = 8, SK = 65536, DH = 256, FF = 1792;
constexpr int NWG = 256, NJ = 32, NWAVE = NC + 3, NTHR = NWAVE * 64;
constexpr int TILE_BYTES = 4096;
constexpr int RANGE_TILES = SK / 8 / NJ;
static_assert(NC >= 4 && NC <= 8 && RT % 8 == 0 && RANGE_TILES % NC == 0 && HD >= 1 && HD <= 12 && (GL == 2 || GL == 4) && WIN % GL == 0 && WIN <= 40, "deal");
constexpr int NRED = 28;
constexpr unsigned SPIN_CAP = 1u << 20, OOB = 0x80000000u;

constexpr unsigned WS_EPOCH = 0, WS_QG = 8192, WS_HP = WS_QG + 4 * 4096 * 4, WS_ML = WS_HP + 8 * FF * 4, WS_P1 = WS_ML + HQ * NJ * 16,
                   WS_Y = WS_P1 + HKV * NJ * NJ * 64, WS_END = WS_Y + NRED * NWG * 64;
constexpr int L_SC = RT * 1024, L_Q = L_SC + RANGE_TILES * 16, L_FULL = L_Q + 256, L_QRDY = L_FULL + 1, L_FIN = L_FULL + 2, L_YRDY = L_FULL + 3, L_FREE = L_FULL + 4, L_CH = L_FULL + 12,
              L_H1 = L_FULL + 20, L_MF = L_FULL + 24, L_TAG = L_FULL + 32, L_MX = L_FULL + 36, L_FINV = L_FULL + 52, L_Y2 = L_FINV + 64, L_REC = L_Y2 + 520, REC = 516,
              L_END = L_REC + (NC - 1) * REC;
static_assert(L_END * 4 <= 160 * 1024, "LDS");

using bf16x2 = __attribute__((ext_vector_type(2))) __bf16;
using u32x2  = __attribute__((ext_vector_type(2))) unsigned;
using u32x3  = __attribute__((ext_vector_type(3))) unsigned;
using u32x4  = __attribute__((ext_vector_type(4))) unsigned;
using f32x2  = __attribute__((ext_vector_type(2))) float;
using f32x4  = __attribute__((ext_vector_type(4))) float;
#define LAS __attribute__((address_space(3)))
#define RLX_WG __ATOMIC_RELAXED, __HIP_MEMORY_SCOPE_WORKGROUP
#define CBAR() asm volatile("" ::: "memory")
#define SB() __builtin_amdgcn_sched_barrier(0)
#define LGKM0() asm volatile("s_waitcnt lgkmcnt(0)" ::: "memory")
#define STR2(x) #x
#define STR(x) STR2(x)
#define RSRC(p, bytes) __builtin_amdgcn_make_buffer_rsrc((void*)(p), 0, (int)(bytes), 0x00020000)
#define LD128(r, off, soff, aux) __builtin_amdgcn_raw_buffer_load_b128(r, off, soff, aux)
#define ST128(v, r, off) __builtin_amdgcn_raw_buffer_store_b128(v, r, off, 0, 16)

__device__ __forceinline__ float dot2(unsigned k, unsigned q, float acc) {
  return __builtin_amdgcn_fdot2_f32_bf16(__builtin_bit_cast(bf16x2, k), __builtin_bit_cast(bf16x2, q), acc, false);
}
template <int CTRL> __device__ __forceinline__ float dpp(float x) {
  return __builtin_bit_cast(float, __builtin_amdgcn_mov_dpp(__builtin_bit_cast(int, x), CTRL, 0xf, 0xf, true));
}
template <int CTRL> __device__ __forceinline__ unsigned dppu(unsigned x) { return (unsigned)__builtin_amdgcn_mov_dpp((int)x, CTRL, 0xf, 0xf, true); }
constexpr int XOR1 = 0xB1, XOR2 = 0x4E, XOR7 = 0x141, XOR8 = 0x128, ROR4 = 0x124;
__device__ __forceinline__ float bflo(unsigned u) { return __builtin_bit_cast(float, u << 16); }
__device__ __forceinline__ float bfhi(unsigned u) { return __builtin_bit_cast(float, u & 0xffff0000u); }
__device__ __forceinline__ unsigned f2bf(float f) { __hip_bfloat16 b = __float2bfloat16(f); return __builtin_bit_cast(unsigned short, b); }
__device__ __forceinline__ float tr16(float a, float b) { auto s = __builtin_amdgcn_permlane16_swap(__float_as_uint(a), __float_as_uint(b), false, false); return __uint_as_float(s[0]) + __uint_as_float(s[1]); }
__device__ __forceinline__ float tr32(float a, float b) { auto s = __builtin_amdgcn_permlane32_swap(__float_as_uint(a), __float_as_uint(b), false, false); return __uint_as_float(s[0]) + __uint_as_float(s[1]); }
__device__ __forceinline__ float sw16_add(float x) { return tr16(x, x); }
__device__ __forceinline__ float sw32_add(float x) { return tr32(x, x); }
__device__ __forceinline__ float sw16_max(float x) { auto s = __builtin_amdgcn_permlane16_swap(__float_as_uint(x), __float_as_uint(x), false, false); return fmaxf(__uint_as_float(s[0]), __uint_as_float(s[1])); }
__device__ __forceinline__ float sw32_max(float x) { auto s = __builtin_amdgcn_permlane32_swap(__float_as_uint(x), __float_as_uint(x), false, false); return fmaxf(__uint_as_float(s[0]), __uint_as_float(s[1])); }
__device__ __forceinline__ float red_q(float x) { x += dpp<ROR4>(x); x += dpp<XOR8>(x); return sw32_add(sw16_add(x)); }
__device__ __forceinline__ float red_all(float x) { x += dpp<XOR1>(x); x += dpp<XOR2>(x); return red_q(x); }
__device__ __forceinline__ float red4(float v0, float v1, float v2, float v3) {
  float u = tr32(tr16(v0, v1), tr16(v2, v3));
  u += dpp<ROR4>(u); u += dpp<XOR8>(u);
  return u;
}
__device__ __forceinline__ unsigned rotl(unsigned x, unsigned r) { return __builtin_amdgcn_alignbit(x, x, (32u - r) & 31u); }
__device__ __forceinline__ unsigned rec_fold(u32x4 w, unsigned r8) {
  unsigned h = rotl(w[0] ^ rotl(w[1], 2) ^ rotl(w[2], 4) ^ rotl(w[3], 6), r8);
  h ^= dppu<XOR1>(h); h ^= dppu<XOR2>(h);
  return h;
}
__device__ __forceinline__ u32x4 rec_seal(f32x4 v, unsigned tag, unsigned r8) {
  u32x4 w = {__float_as_uint(v[0]) & ~3u, __float_as_uint(v[1]) & ~3u, __float_as_uint(v[2]) & ~3u, __float_as_uint(v[3]) & ~3u};
  const unsigned cb = (rec_fold(w, r8) ^ tag) >> r8;
  w[0] |= cb & 3u; w[1] |= (cb >> 2) & 3u; w[2] |= (cb >> 4) & 3u; w[3] |= (cb >> 6) & 3u;
  return w;
}
__device__ __forceinline__ float rec_val(unsigned w) { return __uint_as_float(w & ~3u); }
__device__ __forceinline__ f32x4 proj_reduce(const f32x2 (&a)[4], int lane) {
  f32x4 r;
#pragma unroll
  for (int k = 0; k < 4; ++k) {
    const float lo = a[k >> 1][k & 1], hi = a[2 + (k >> 1)][k & 1];
    const float t1 = lo + dpp<XOR1>(lo), t2 = hi + dpp<XOR1>(hi);
    r[k] = sw32_add(sw16_add((lane & 1) ? t2 : t1));
  }
  return r;
}
__device__ __forceinline__ unsigned now() { return (unsigned)__builtin_amdgcn_s_memrealtime(); }
__device__ __forceinline__ unsigned lds_peek(unsigned byte_addr) {
  unsigned v;
  asm volatile("ds_read_b32 %0, %1\n\ts_waitcnt lgkmcnt(0)" : "=v"(v) : "v"(byte_addr) : "memory");
  return __builtin_amdgcn_readfirstlane(v);
}
__device__ __forceinline__ void lds_poke(unsigned byte_addr, unsigned v) { asm volatile("ds_write_b32 %0, %1" :: "v"(byte_addr), "v"(v) : "memory"); }
__device__ __forceinline__ void lds_wait(unsigned byte_addr, unsigned atleast) {
  for (unsigned spins = 0; lds_peek(byte_addr) < atleast && spins < SPIN_CAP; ++spins) __builtin_amdgcn_s_sleep(1);
}

__global__ void __launch_bounds__(NTHR, (NWAVE + 3) / 4)
k_fused(const unsigned char* __restrict__ Kc, const unsigned char* __restrict__ Vc, const unsigned short* __restrict__ x,
        const unsigned short* __restrict__ wq, const unsigned short* __restrict__ wo, const unsigned short* __restrict__ w1,
        const unsigned short* __restrict__ b1, const unsigned short* __restrict__ w2, const unsigned short* __restrict__ b2,
        const unsigned short* __restrict__ gw, unsigned short* __restrict__ out, unsigned char* ws) {
  __shared__ __attribute__((aligned(16))) unsigned lds[L_END];
  const unsigned lbase = (unsigned)(size_t)(LAS unsigned*)lds;
  const int b = blockIdx.x;
  const int tid = threadIdx.x, lane = tid & 63, wave = __builtin_amdgcn_readfirstlane(tid >> 6);

  if (__builtin_expect(wave == 0, 1)) {
    __builtin_amdgcn_s_setprio(3);
    const auto rk = RSRC(Kc, HKV * SK * DH * 2);
    const auto rv = RSRC(Vc, HKV * SK * DH * 2);
    const unsigned voff = lane * 16;
    unsigned off = (unsigned)b * (unsigned)(RANGE_TILES * TILE_BYTES), slot = 0;
    const unsigned fulla = lbase + L_FULL * 4, ctla = lbase + (L_FREE + (lane < NC ? lane : 0)) * 4;
    int issued = 0;
#define DMA(R, S, O) __builtin_amdgcn_raw_ptr_buffer_load_lds(R, (LAS void*)((LAS unsigned char*)lds + (S)), 16, voff, off, O, 2)
#define PUB() do { asm volatile("s_waitcnt vmcnt(" STR(WIN - GL) ")" ::: "memory"); lds_poke(fulla, max(lmax, issued - (WIN - GL))); issued += GL; } while (0)
#define PART(R, S) do { PUB(); DMA(R, S, 0); DMA(R, S, 1024); if (GL == 2) PUB(); DMA(R, S, 2048); DMA(R, S, 3072); } while (0)
#define TILE(R) do { PART(R, slot); off += TILE_BYTES; slot += TILE_BYTES; } while (0)
    int lmax = 0;
    DMA(rk, 0, 0); DMA(rk, 0, 1024); DMA(rk, 0, 2048); DMA(rk, 0, 3072);
    off += TILE_BYTES;
    DMA(rk, TILE_BYTES, 0); DMA(rk, TILE_BYTES, 1024); DMA(rk, TILE_BYTES, 2048); DMA(rk, TILE_BYTES, 3072);
    off += TILE_BYTES; slot = 2 * TILE_BYTES; issued = 8;
    if (lane < 32) lds_poke(fulla + lane * 4, 0u);
    TILE(rk); TILE(rk);
    LGKM0();
    __builtin_amdgcn_s_barrier();
    TILE(rk); TILE(rk); TILE(rk); TILE(rk);
#define RINGWAIT(t) do { \
      if (slot == RT * TILE_BYTES) slot = 0; \
      const unsigned need = (t) + 8 > RT ? (unsigned)((t) + 8 - RT + NC - 1) / NC : 0u; \
      unsigned v; \
      asm volatile("ds_read_b32 %0, %1\n\ts_waitcnt lgkmcnt(0)" : "=v"(v) : "v"(ctla) : "memory"); \
      if (__any(v < need)) { \
        asm volatile("s_waitcnt vmcnt(0)" ::: "memory"); \
        lmax = issued; lds_poke(fulla, issued); \
        unsigned spins = 0; \
        do { __builtin_amdgcn_s_sleep(1); asm volatile("ds_read_b32 %0, %1\n\ts_waitcnt lgkmcnt(0)" : "=v"(v) : "v"(ctla) : "memory"); } while (__any(v < need) && ++spins < SPIN_CAP); \
      } } while (0)
    for (int t = 8; t < RANGE_TILES; t += 8) {
      RINGWAIT(t);
      TILE(rk); TILE(rk); TILE(rk); TILE(rk); TILE(rk); TILE(rk); TILE(rk); TILE(rk);
    }
    off -= RANGE_TILES * TILE_BYTES;
    for (int t = RANGE_TILES; t < 2 * RANGE_TILES; t += 8) {
      RINGWAIT(t);
      TILE(rv); TILE(rv); TILE(rv); TILE(rv); TILE(rv); TILE(rv); TILE(rv); TILE(rv);
    }
#define TAILPUB(n) if (WIN > (n)) { asm volatile("s_waitcnt vmcnt(" #n ")" ::: "memory"); lds_poke(fulla, RANGE_TILES * 8 - (n)); }
    TAILPUB(36) TAILPUB(32) TAILPUB(28) TAILPUB(24) TAILPUB(20) TAILPUB(16) TAILPUB(12) TAILPUB(8) TAILPUB(4) TAILPUB(0)
    return;
  }

  const int g = b >> 5, j = b & 31;
  const unsigned r8 = 8 * (lane & 3);
  const auto rws = RSRC(ws, WS_END);

  if (wave >= 3) {
    const int sw = wave - 3, c = lane & 31, t = lane & 15;
    constexpr float C2 = 0.0625f * 1.4426950408889634f;
    __builtin_amdgcn_s_barrier();
    u32x4 q0, q1;
    float m = -INFINITY, l = 0.f;
    float x_[8];
    unsigned full = 0, tag;
    float* scl = reinterpret_cast<float*>(&lds[L_SC + sw * 16 + (lane >> 5) * 8 + (t >> 1)]);
#define WAITFULL(n) do { for (unsigned spins = 0; full < (unsigned)(n) && spins < SPIN_CAP; ++spins) { full = lds_peek(lbase + L_FULL * 4); if (full >= (unsigned)(n)) break; __builtin_amdgcn_s_sleep(1); } } while (0)
#define RD4(dst, tile) do { const u32x4* src_ = reinterpret_cast<const u32x4*>(&lds[((tile) % RT) * 1024 + lane * 4]); \
      CBAR(); dst[0] = src_[0]; dst[1] = src_[64]; dst[2] = src_[128]; dst[3] = src_[192]; } while (0)
#define PIN4(a) asm volatile("" : "+v"(a[0]), "+v"(a[1]), "+v"(a[2]), "+v"(a[3]) :: "memory")
#define RELEASE(n) do { LGKM0(); lds_poke(lbase + (L_FREE + sw) * 4, (n)); } while (0)
#define QK1(KK, i) { float a_ = 0.f, b_ = 0.f; \
      _Pragma("unroll") for (int k = 0; k < 4; ++k) { a_ = dot2((KK)[k], q0[k], a_); b_ = dot2((KK)[k], q1[k], b_); } \
      x_[2 * (i)] = a_; x_[2 * (i) + 1] = b_; }
#define TR_STEP(HALF, CTRL, BIT) _Pragma("unroll") for (int h = 0; h < (HALF); ++h) { \
      const float lo_ = x_[h], hi_ = x_[h + (HALF)]; const float t1_ = lo_ + dpp<CTRL>(lo_), t2_ = hi_ + dpp<CTRL>(hi_); \
      x_[h] = (t & (BIT)) ? t2_ : t1_; }
#define SCORES(KB, n) { QK1(KB[0], 0) QK1(KB[1], 1) QK1(KB[2], 2) QK1(KB[3], 3) \
      TR_STEP(4, XOR8, 8) TR_STEP(2, XOR7, 4) TR_STEP(1, XOR2, 2) x_[0] += dpp<XOR1>(x_[0]); \
      const float sc = sw16_add(x_[0]) * C2; \
      m = fmaxf(m, sc); \
      if ((lane & 17) == 0) scl[(n) * NC * 16] = sc; }
#define PV1(VV, i) { const float p0 = dpp<0x150 + 4 * (i)>(p), p1 = dpp<0x150 + 4 * (i) + 2>(p); \
      _Pragma("unroll") for (int k = 0; k < 4; ++k) { const f32x2 vf = {bflo((VV)[k]), bfhi((VV)[k])}; \
        acc0[k] = __builtin_elementwise_fma(f32x2{p0, p0}, vf, acc0[k]); acc1[k] = __builtin_elementwise_fma(f32x2{p1, p1}, vf, acc1[k]); } }
#define PINACC() _Pragma("unroll") for (int k = 0; k < 4; ++k) { asm volatile("" : "+v"(acc0[k])); asm volatile("" : "+v"(acc1[k])); }
    {
      {
        u32x4 Kb[HD][4];
#pragma unroll
        for (int d = 0; d < HD; ++d) {
          const int tile = d * NC + sw;
          WAITFULL(tile * 4 + 4);
          RD4(Kb[d], tile);
          RELEASE(d + 1);
          PIN4(Kb[d]);
        }
        lds_wait(lbase + L_QRDY * 4, 1u);
        q0 = *reinterpret_cast<const u32x4*>(&lds[L_Q + 4 * c]); q1 = *reinterpret_cast<const u32x4*>(&lds[L_Q + 128 + 4 * c]);
        tag = lds_peek(lbase + L_TAG * 4);
#pragma unroll
        for (int d = 0; d < HD; ++d) { SCORES(Kb[d], d) SB(); }
      }
      for (int n = HD; n < RANGE_TILES / NC; ++n) {
        const int tile = n * NC + sw;
        u32x4 Kb[4];
        WAITFULL(tile * 4 + 4);
        RD4(Kb, tile);
        RELEASE(n + 1);
        PIN4(Kb);
        SCORES(Kb, n)
        SB();
      }
      m = fmaxf(m, dpp<ROR4>(m)); m = fmaxf(m, dpp<XOR8>(m)); m = sw32_max(m);
      {
        float* mx = reinterpret_cast<float*>(&lds[L_MX]);
        if (lane == 0 || lane == 2) mx[sw * 2 + (lane >> 1)] = m;
        LGKM0();
        lds_poke(lbase + (L_MF + sw) * 4, 1u);
        for (unsigned spins = 0; spins < SPIN_CAP; ++spins) {
          unsigned fl;
          asm volatile("ds_read_b32 %0, %1\n\ts_waitcnt lgkmcnt(0)" : "=v"(fl) : "v"(lbase + (L_MF + (lane < NC ? lane : 0)) * 4) : "memory");
          if (__all(fl != 0u)) break;
          __builtin_amdgcn_s_sleep(1);
        }
#pragma unroll
        for (int w = 0; w < NC; ++w) m = fmaxf(m, mx[w * 2 + ((t >> 1) & 1)]);
      }
      f32x2 acc0[4], acc1[4];
#pragma unroll
      for (int k = 0; k < 4; ++k) { acc0[k] = f32x2{0.f, 0.f}; acc1[k] = f32x2{0.f, 0.f}; }
      for (int n = 0; n < RANGE_TILES / NC; ++n) {
        const int tile = RANGE_TILES + n * NC + sw;
        u32x4 Vb[4];
        const float p = __builtin_amdgcn_exp2f(scl[n * NC * 16] - m);
        l += p;
        WAITFULL(tile * 4 + 4);
        RD4(Vb, tile);
        RELEASE(RANGE_TILES / NC + n + 1);
        PIN4(Vb);
        PV1(Vb[0], 0) PV1(Vb[1], 1) PV1(Vb[2], 2) PV1(Vb[3], 3)
        PINACC(); SB();
      }
      f32x4 ra, rb;
#pragma unroll
      for (int k = 0; k < 2; ++k)
#pragma unroll
        for (int e = 0; e < 2; ++e) { ra[2 * k + e] = tr32(acc0[k][e], acc1[k][e]); rb[2 * k + e] = tr32(acc0[2 + k][e], acc1[2 + k][e]); }
      float lt = l; lt += dpp<ROR4>(lt); lt += dpp<XOR8>(lt); lt = sw32_add(lt);
      const float m0 = dpp<0x00>(m), m1 = dpp<0xAA>(m), l0 = dpp<0x00>(lt), l1 = dpp<0xAA>(lt);
      const float mh = lane < 32 ? m0 : m1;
      float lh = lane < 32 ? l0 : l1;
      if (sw == NC - 1) {
        for (unsigned spins = 0; spins < SPIN_CAP; ++spins) {
          unsigned fl;
          asm volatile("ds_read_b32 %0, %1\n\ts_waitcnt lgkmcnt(0)" : "=v"(fl) : "v"(lbase + (L_CH + (lane < NC - 1 ? lane : 0)) * 4) : "memory");
          if (__all(fl != 0u)) break;
        }
#pragma unroll
        for (int w = 0; w < NC - 1; ++w) {
          const float* pr = reinterpret_cast<const float*>(&lds[L_REC + w * REC]);
          ra += *reinterpret_cast<const f32x4*>(pr + 4 + 8 * lane); rb += *reinterpret_cast<const f32x4*>(pr + 8 + 8 * lane);
          lh += pr[lane >> 5];
        }
      }
      if (sw < NC - 1) {
        float* rec = reinterpret_cast<float*>(&lds[L_REC + sw * REC]);
        *reinterpret_cast<f32x4*>(rec + 4 + 8 * lane) = ra;
        *reinterpret_cast<f32x4*>(rec + 8 + 8 * lane) = rb;
        if (c == 0) rec[lane >> 5] = lh;
        LGKM0();
        lds_poke(lbase + (L_CH + sw) * 4, 1u);
      } else {
        const unsigned po = WS_P1 + ((g * NJ + 2 * (lane >> 2)) * NJ + j) * 64 + (lane & 3) * 16;
        ST128(rec_seal(ra, tag, r8), rws, po);
        ST128(rec_seal(rb, tag, r8), rws, po + NJ * 64);
        if (c == 0) ST128((u32x4{__float_as_uint(mh), __float_as_uint(lh), __float_as_uint(mh) ^ rotl(__float_as_uint(lh), 16) ^ tag, 0u}), rws, WS_ML + ((g * 2 + (lane >> 5)) * NJ + j) * 16);
      }
      if (b < NRED && sw < 4) {
        u32x4 v[4], w_[4]; unsigned spins = 0u;
        const unsigned ybase = WS_Y + (b * NWG + sw * 64) * 64 + lane * 16;
        lds_wait(lbase + L_H1 * 4, 1u);
#define YLOAD(dst) do { CBAR(); _Pragma("unroll") for (int i = 0; i < 4; ++i) dst[i] = LD128(rws, ybase + i * 1024, 0, 16); CBAR(); } while (0)
#define YOK(src) __all((((rec_fold(src[0], r8) ^ tag) | (rec_fold(src[1], r8) ^ tag)) | ((rec_fold(src[2], r8) ^ tag) | (rec_fold(src[3], r8) ^ tag))) == 0u)
        YLOAD(v);
        __builtin_amdgcn_s_sleep(PIPE);
        for (;;) {
          YLOAD(w_);
          if (YOK(v)) break;
          YLOAD(v);
          if (YOK(w_) || ++spins > SPIN_CAP) {
#pragma unroll
            for (int i = 0; i < 4; ++i) v[i] = w_[i];
            break;
          }
        }
        float s[4];
#pragma unroll
        for (int k = 0; k < 4; ++k) s[k] = (rec_val(v[0][k]) + rec_val(v[1][k])) + (rec_val(v[2][k]) + rec_val(v[3][k]));
        const float u = red4(s[0], s[1], s[2], s[3]);
        float* fin = reinterpret_cast<float*>(&lds[L_FINV]);
        if ((lane & 12) == 0) fin[sw * 16 + (lane & 3) * 4 + (lane >> 4)] = u;
        unsigned old = 0;
        CBAR();
        if (lane == 0) old = __hip_atomic_fetch_add((LAS unsigned*)&lds[L_FIN], 1u, RLX_WG);
        old = __builtin_amdgcn_readfirstlane(old);
        CBAR();
        if (old == 3u) {
          if (lane < 16) out[b * 16 + lane] = (unsigned short)f2bf((fin[lane] + fin[16 + lane]) + (fin[32 + lane] + fin[48 + lane]));
          if (b == 0 && lane == 0) __builtin_amdgcn_raw_buffer_store_b64(u32x2{lds[L_TAG + 1], lds[L_TAG + 2]}, rws, WS_EPOCH, 0, 16);
        }
      }
    }
    return;
  }

  const int cw = wave - 1;
  const unsigned tc0 = now();
  const int rr = (lane & 1) | ((lane >> 4) << 1);
  unsigned tag;
  if (cw == 0) {
    const int qcg = b >> 2, qrg = b & 3, row0 = qrg * 112 + rr * 14;
    const auto rx = RSRC(x, DM * 2);
    const auto rq = RSRC(wq, DM * HQ * DH * 2);
    u32x4 w[14];
#pragma unroll
    for (int i = 0; i < 14; ++i) w[i] = LD128(rq, row0 * (HQ * DH * 2) + qcg * 128 + (lane & 14) * 8, i * (HQ * DH * 2), 2);
    const u32x4 xa = LD128(rx, row0 * 2, 0, 0);
    const u32x3 xb = __builtin_amdgcn_raw_buffer_load_b96(rx, row0 * 2 + 16, 0, 0);
    const u32x2 ep = __builtin_amdgcn_raw_buffer_load_b64(rws, WS_EPOCH, 0, 16);
    SB();
    __builtin_amdgcn_s_barrier();
    SB();
    const unsigned xw[7] = {xa[0], xa[1], xa[2], xa[3], xb[0], xb[1], xb[2]};
    f32x2 a[4];
#pragma unroll
    for (int k = 0; k < 4; ++k) a[k] = f32x2{0.f, 0.f};
#pragma unroll
    for (int i = 0; i < 14; ++i) {
      const float xv = (i & 1) ? bfhi(xw[i >> 1]) : bflo(xw[i >> 1]);
#pragma unroll
      for (int k = 0; k < 4; ++k) a[k] = __builtin_elementwise_fma(f32x2{xv, xv}, f32x2{bflo(w[i][k]), bfhi(w[i][k])}, a[k]);
    }
    const unsigned long long tg = (((unsigned long long)ep[1] << 32) | ep[0]) + 1ull;
    tag = 2u * (unsigned)tg + 1u;
    {
      const u32x4 rec = rec_seal(proj_reduce(a, lane), tag, r8);
      if (lane < 16) ST128(rec, rws, WS_QG + (qrg * 4096 + qcg * 64) * 4 + lane * 16);
    }
    u32x4 gq[8]; unsigned go[8], spins = 0;
#pragma unroll
    for (int i = 0; i < 8; ++i) { go[i] = WS_QG + ((i >> 1) * 4096 + g * 512 + (i & 1) * 256) * 4 + lane * 16; gq[i] = u32x4{0u, 0u, 0u, 0u}; }
    while (now() - tc0 < QPOLL_T) __builtin_amdgcn_s_sleep(2);
    for (;;) {
      CBAR();
      u32x4 t_[8];
#pragma unroll
      for (int i = 0; i < 8; ++i) t_[i] = LD128(rws, go[i], 0, 16);
      bool all = true;
#pragma unroll
      for (int i = 0; i < 8; ++i) {
        if (rec_fold(t_[i], r8) == tag) { gq[i] = t_[i]; go[i] = OOB; }
        all &= go[i] == OOB;
      }
      if (__all(all) || ++spins > SPIN_CAP) break;
      __builtin_amdgcn_s_sleep(8);
    }
#pragma unroll
    for (int hf = 0; hf < 2; ++hf) {
      float qs[4];
#pragma unroll
      for (int k = 0; k < 4; ++k) qs[k] = (rec_val(gq[hf][k]) + rec_val(gq[2 + hf][k])) + (rec_val(gq[4 + hf][k]) + rec_val(gq[6 + hf][k]));
      *reinterpret_cast<u32x2*>(&lds[L_Q + hf * 128 + 2 * lane]) = u32x2{f2bf(qs[0]) | (f2bf(qs[1]) << 16), f2bf(qs[2]) | (f2bf(qs[3]) << 16)};
    }
    if (lane == 0) { lds[L_TAG] = tag; lds[L_TAG + 1] = (unsigned)tg; lds[L_TAG + 2] = (unsigned)(tg >> 32); }
    LGKM0();
    lds_poke(lbase + L_QRDY * 4, 1u);
  } else {
    __builtin_amdgcn_s_barrier();
    lds_wait(lbase + L_QRDY * 4, 1u);
    tag = lds_peek(lbase + L_TAG * 4);
    const bool mlp = b < 224;
    const auto rx = RSRC(x, DM * 2);
    const auto rg = RSRC(gw, DM * 2);
    const auto rb2 = RSRC(b2, DM * 2);
    const auto r1 = RSRC(w1, mlp ? DM * FF * 2 : 0);
    const auto r2 = RSRC(w2, mlp ? FF * DM * 2 : 0);
    const auto rb = RSRC(b1, FF * 2);
    {
      const int row0 = (b & 7) * 56 + rr * 7;
      u32x4 w1v[7]; unsigned short xr[7];
#pragma unroll
      for (int i = 0; i < 7; ++i) { w1v[i] = LD128(r1, (row0 + i) * (FF * 2) + (b >> 3) * 128 + (lane & 14) * 8, 0, 2); xr[i] = x[row0 + i]; }
      f32x2 hv[4];
#pragma unroll
      for (int k = 0; k < 4; ++k) hv[k] = f32x2{0.f, 0.f};
#pragma unroll
      for (int i = 0; i < 7; ++i) {
        const float xf = bflo(xr[i]);
#pragma unroll
        for (int k = 0; k < 4; ++k) hv[k] = __builtin_elementwise_fma(f32x2{xf, xf}, f32x2{bflo(w1v[i][k]), bfhi(w1v[i][k])}, hv[k]);
      }
      const u32x4 rec = rec_seal(proj_reduce(hv, lane), tag, r8);
      if (mlp && lane < 16) ST128(rec, rws, WS_HP + ((b & 7) * FF + (b >> 3) * 64) * 4 + lane * 16);
    }
    const int hc0 = (mlp ? b : 0) * 8;
    const u32x4 xv = LD128(rx, lane * 16, 0, 0), gv = LD128(rg, lane * 16, 0, 0), b2v = LD128(rb2, lane * 16, 0, 0);
    const u32x4 bv = LD128(rb, hc0 * 2, 0, 0);
    u32x4 wb[8];
#pragma unroll
    for (int i = 0; i < 8; ++i) wb[i] = LD128(r2, (hc0 + i) * (DM * 2) + lane * 16, 0, 2);
    float s = 0.f;
#pragma unroll
    for (int k = 0; k < 4; ++k) { s = fmaf(bflo(xv[k]), bflo(gv[k]), s); s = fmaf(bfhi(xv[k]), bfhi(gv[k]), s); }
    s = red_all(s);
    const float gate = 1.f / (1.f + __expf(-s));
    const float bsc = b == NWG - 1 ? 1.f - gate : 0.f;
    f32x2 y2[4];
#pragma unroll
    for (int k = 0; k < 4; ++k) y2[k] = f32x2{bsc * bflo(b2v[k]), bsc * bfhi(b2v[k])};
    if (mlp) {
      u32x4 hr; unsigned spins = 0;
      while (now() - tc0 < HPOLL_T) __builtin_amdgcn_s_sleep(16);
      for (;;) {
        CBAR();
        hr = LD128(rws, WS_HP + (((lane >> 2) & 7) * FF + (b >> 1) * 16) * 4 + (lane & 3) * 16, 0, 16);
        if (__all(rec_fold(hr, r8) == tag) || ++spins > SPIN_CAP) break;
        __builtin_amdgcn_s_sleep(16);
      }
      float hs[4];
#pragma unroll
      for (int k = 0; k < 4; ++k) { float v_ = rec_val(hr[k]); v_ += dpp<ROR4>(v_); v_ += dpp<XOR8>(v_); hs[k] = sw16_add(v_); }
#pragma unroll
      for (int k = 0; k < 8; ++k) {
        const float h = __uint_as_float(__builtin_amdgcn_readlane(__float_as_uint(hs[k & 3]), (b & 1) * 2 + (k >> 2))) + ((k & 1) ? bfhi(bv[k >> 1]) : bflo(bv[k >> 1]));
        const float u = 0.7978845608028654f * (h + 0.044715f * h * h * h);
        const float ge = 0.5f * h * (1.f + tanhf(u)) * (1.f - gate);
#pragma unroll
        for (int k2 = 0; k2 < 4; ++k2) y2[k2] = __builtin_elementwise_fma(f32x2{ge, ge}, f32x2{bflo(wb[k][k2]), bfhi(wb[k][k2])}, y2[k2]);
      }
    }
    float* yl = reinterpret_cast<float*>(&lds[L_Y2]);
    *reinterpret_cast<f32x4*>(yl + 8 * lane) = f32x4{y2[0][0], y2[0][1], y2[1][0], y2[1][1]};
    *reinterpret_cast<f32x4*>(yl + 8 * lane + 4) = f32x4{y2[2][0], y2[2][1], y2[3][0], y2[3][1]};
    if (lane == 0) yl[512] = gate;
    LGKM0();
    lds_poke(lbase + L_YRDY * 4, 1u);
  }
  f32x2 wof[16][2];
  {
    u32x2 wov[16];
    const auto ro = RSRC(wo, HQ * DH * DM * 2);
    const int d0 = g * 512 + (j >> 1) * 32 + (j & 1) * 4;
#pragma unroll
    for (int r = 0; r < 16; ++r) wov[r] = __builtin_amdgcn_raw_buffer_load_b64(ro, (d0 + (r >> 2) * 8 + (r & 3)) * (DM * 2) + cw * 448 + lane * 8, 0, 2);
#pragma unroll
    for (int r = 0; r < 16; ++r)
#pragma unroll
      for (int k = 0; k < 2; ++k) { wof[r][k] = f32x2{bflo(wov[r][k]), bfhi(wov[r][k])}; asm volatile("" : "+v"(wof[r][k])); }
  }
  lds_wait(lbase + L_YRDY * 4, 1u);
  const float* yl = reinterpret_cast<const float*>(&lds[L_Y2]);
  const f32x4 ya = *reinterpret_cast<const f32x4*>(yl + 224 * cw + 4 * lane);
  const float gate = yl[512];
  {
    for (unsigned spins = 0; lds_peek(lbase + L_FULL * 4) < (unsigned)(RANGE_TILES * 8) && spins < SPIN_CAP; ++spins) __builtin_amdgcn_s_sleep(4);
    u32x4 pa, pb, ma, mb, pa2, pb2, ma2, mb2; unsigned spins = 0u;
    const unsigned pbase = WS_P1 + (g * NJ + j) * NJ * 64 + lane * 16, mbase = WS_ML + ((g * 2 + (j >> 4)) * NJ + (lane >> 2)) * 16;
#define HLOAD(A, B, C, D) do { CBAR(); A = LD128(rws, pbase, 0, 16); B = LD128(rws, pbase + 1024, 0, 16); C = LD128(rws, mbase, 0, 16); D = LD128(rws, mbase + 256, 0, 16); CBAR(); } while (0)
#define HBAD(A, B, C, D) __builtin_amdgcn_ballot_w64((((rec_fold(A, r8) ^ tag) | (rec_fold(B, r8) ^ tag)) | ((C[0] ^ rotl(C[1], 16) ^ C[2] ^ tag) | (D[0] ^ rotl(D[1], 16) ^ D[2] ^ tag))) != 0u)
    unsigned long long bad;
    for (;;) {
      HLOAD(pa, pb, ma, mb);
      bad = HBAD(pa, pb, ma, mb);
      if (__builtin_popcountll(bad) <= LAZYN || ++spins > SPIN_CAP) break;
      __builtin_amdgcn_s_sleep(LAZY);
    }
    if (bad) {
      HLOAD(pa, pb, ma, mb);
      __builtin_amdgcn_s_sleep(PIPE);
      for (;;) {
        HLOAD(pa2, pb2, ma2, mb2);
        if (!HBAD(pa, pb, ma, mb)) break;
        HLOAD(pa, pb, ma, mb);
        if (!HBAD(pa2, pb2, ma2, mb2) || ++spins > SPIN_CAP) { pa = pa2; pb = pb2; ma = ma2; mb = mb2; break; }
      }
    }
    const float m_a = __uint_as_float(ma[0]), m_b = __uint_as_float(mb[0]);
    float M = fmaxf(m_a, m_b);
    M = fmaxf(M, dpp<ROR4>(M)); M = fmaxf(M, dpp<XOR8>(M)); M = sw32_max(sw16_max(M));
    const float wa = __builtin_amdgcn_exp2f(m_a - M), wb = __builtin_amdgcn_exp2f(m_b - M);
    const float L = red_q(wa * __uint_as_float(ma[1]) + wb * __uint_as_float(mb[1]));
    const float sc = gate * __builtin_amdgcn_rcpf(L);
    const float u = red4(wa * rec_val(pa[0]) + wb * rec_val(pb[0]), wa * rec_val(pa[1]) + wb * rec_val(pb[1]),
                         wa * rec_val(pa[2]) + wb * rec_val(pb[2]), wa * rec_val(pa[3]) + wb * rec_val(pb[3]));
    f32x2 y[2], z[2];
#define OROW(r) __uint_as_float(__builtin_amdgcn_readlane(__float_as_uint(u), 16 * ((r) & 3) + ((r) >> 2)))
    { const float o0 = OROW(0), o1 = OROW(1); y[0] = o0 * wof[0][0]; y[1] = o0 * wof[0][1]; z[0] = o1 * wof[1][0]; z[1] = o1 * wof[1][1]; }
#define YR2(r) { const float o0 = OROW(r), o1 = OROW((r) + 1); \
      y[0] = __builtin_elementwise_fma(f32x2{o0, o0}, wof[r][0], y[0]); y[1] = __builtin_elementwise_fma(f32x2{o0, o0}, wof[r][1], y[1]); \
      z[0] = __builtin_elementwise_fma(f32x2{o1, o1}, wof[(r) + 1][0], z[0]); z[1] = __builtin_elementwise_fma(f32x2{o1, o1}, wof[(r) + 1][1], z[1]); }
    YR2(2) YR2(4) YR2(6) YR2(8) YR2(10) YR2(12) YR2(14)
    y[0] += z[0]; y[1] += z[1];
    const u32x4 yv = rec_seal(f32x4{fmaf(sc, y[0][0], ya[0]), fmaf(sc, y[0][1], ya[1]), fmaf(sc, y[1][0], ya[2]), fmaf(sc, y[1][1], ya[3])}, tag, r8);
    if (lane < 56) ST128(yv, rws, WS_Y + (((cw * 14 + (lane >> 2)) * NWG + b) * 64 + (lane & 3) * 16));
    if (cw == 0) { lds_poke(lbase + L_H1 * 4, 1u); }
  }
}

extern "C" void kernel_launch(void* const* d_in, const int* in_sizes, int n_in, void* d_out, int out_size, void* d_ws, size_t ws_size, hipStream_t stream) {
  k_fused<<<NWG, NTHR, 0, stream>>>((const unsigned char*)d_in[1], (const unsigned char*)d_in[2], (const unsigned short*)d_in[0], (const unsigned short*)d_in[3],
                                    (const unsigned short*)d_in[4], (const unsigned short*)d_in[6], (const unsigned short*)d_in[7], (const unsigned short*)d_in[8],
                                    (const unsigned short*)d_in[9], (const unsigned short*)d_in[10], (unsigned short*)d_out, (unsigned char*)d_ws);
}
```

```cpp
#include <hip/hip_runtime.h>
#include <hip/hip_bf16.h>
#include <cstdint>

#define NC 4
#define HD 10
#define RT 32
#define GL 4
#define WIN 20
#define LAZY 12
#define LAZYN 16
#define PIPE 5
#define HPOLL_T 1500
#define QPOLL_T 400
#define MLPOLL_T 500
#define B0FIRST 1
#define EARLYW 1

constexpr int DM = 448, HQ = 16, HKV = 8, SK = 65536, DH = 256, FF = 1792;
constexpr int NWG = 256, NJ = 32, NWAVE = NC + 3, NTHR = NWAVE * 64;
constexpr int TILE_BYTES = 4096;
constexpr int RANGE_TILES = SK / 8 / NJ;
static_assert(NC == 4 && RT % 8 == 0 && RANGE_TILES % NC == 0 && HD >= 1 && HD <= 12 && (GL == 1 || GL == 2 || GL == 4) && WIN % GL == 0 && WIN <= 40, "deal");
constexpr int NRED = 28;
constexpr unsigned SPIN_CAP = 1u << 20, OOB = 0x80000000u;

constexpr unsigned WS_EPOCH = 0, WS_QG = 8192, WS_HP = WS_QG + 4 * 4096 * 4, WS_ML = WS_HP + 8 * FF * 4, WS_P1 = WS_ML + HQ * NJ * 16,
                   WS_Y = WS_P1 + HKV * NJ * NJ * 64, WS_END = WS_Y + NRED * NWG * 64;
constexpr int L_SC = RT * 1024, L_Q = L_SC + RANGE_TILES * 16, L_FULL = L_Q + 256, L_QRDY = L_FULL + 1, L_FIN = L_FULL + 2, L_YRDY = L_FULL + 3, L_FREE = L_FULL + 4,
              L_PF = L_FULL + 12, L_H1 = L_FULL + 20, L_WRDY = L_FULL + 21, L_MF = L_FULL + 24, L_TAG = L_FULL + 32, L_MX = L_FULL + 36, L_LS = L_FULL + 52,
              L_WS = L_FULL + 68, L_FINV = L_FULL + 72, L_Y2 = L_FINV + 64, L_END = L_Y2 + 520;
static_assert(L_END * 4 <= 160 * 1024, "LDS");

using bf16x2 = __attribute__((ext_vector_type(2))) __bf16;
using u32x2  = __attribute__((ext_vector_type(2))) unsigned;
using u32x3  = __attribute__((ext_vector_type(3))) unsigned;
using u32x4  = __attribute__((ext_vector_type(4))) unsigned;
using f32x2  = __attribute__((ext_vector_type(2))) float;
using f32x4  = __attribute__((ext_vector_type(4))) float;
#define LAS __attribute__((address_space(3)))
#define RLX_WG __ATOMIC_RELAXED, __HIP_MEMORY_SCOPE_WORKGROUP
#define CBAR() asm volatile("" ::: "memory")
#define SB() __builtin_amdgcn_sched_barrier(0)
#define LGKM0() asm volatile("s_waitcnt lgkmcnt(0)" ::: "memory")
#define STR2(x) #x
#define STR(x) STR2(x)
#define RSRC(p, bytes) __builtin_amdgcn_make_buffer_rsrc((void*)(p), 0, (int)(bytes), 0x00020000)
#define LD128(r, off, soff, aux) __builtin_amdgcn_raw_buffer_load_b128(r, off, soff, aux)
#define ST128(v, r, off) __builtin_amdgcn_raw_buffer_store_b128(v, r, off, 0, 16)

__device__ __forceinline__ float dot2(unsigned k, unsigned q, float acc) {
  return __builtin_amdgcn_fdot2_f32_bf16(__builtin_bit_cast(bf16x2, k), __builtin_bit_cast(bf16x2, q), acc, false);
}
template <int CTRL> __device__ __forceinline__ float dpp(float x) {
  return __builtin_bit_cast(float, __builtin_amdgcn_mov_dpp(__builtin_bit_cast(int, x), CTRL, 0xf, 0xf, true));
}
template <int CTRL> __device__ __forceinline__ unsigned dppu(unsigned x) { return (unsigned)__builtin_amdgcn_mov_dpp((int)x, CTRL, 0xf, 0xf, true); }
constexpr int XOR1 = 0xB1, XOR2 = 0x4E, XOR7 = 0x141, XOR8 = 0x128, ROR4 = 0x124;
__device__ __forceinline__ float bflo(unsigned u) { return __builtin_bit_cast(float, u << 16); }
__device__ __forceinline__ float bfhi(unsigned u) { return __builtin_bit_cast(float, u & 0xffff0000u); }
__device__ __forceinline__ unsigned f2bf(float f) { __hip_bfloat16 b = __float2bfloat16(f); return __builtin_bit_cast(unsigned short, b); }
__device__ __forceinline__ float tr16(float a, float b) { auto s = __builtin_amdgcn_permlane16_swap(__float_as_uint(a), __float_as_uint(b), false, false); return __uint_as_float(s[0]) + __uint_as_float(s[1]); }
__device__ __forceinline__ float tr32(float a, float b) { auto s = __builtin_amdgcn_permlane32_swap(__float_as_uint(a), __float_as_uint(b), false, false); return __uint_as_float(s[0]) + __uint_as_float(s[1]); }
__device__ __forceinline__ float sw16_add(float x) { return tr16(x, x); }
__device__ __forceinline__ float sw32_add(float x) { return tr32(x, x); }
__device__ __forceinline__ float sw16_max(float x) { auto s = __builtin_amdgcn_permlane16_swap(__float_as_uint(x), __float_as_uint(x), false, false); return fmaxf(__uint_as_float(s[0]), __uint_as_float(s[1])); }
__device__ __forceinline__ float sw32_max(float x) { auto s = __builtin_amdgcn_permlane32_swap(__float_as_uint(x), __float_as_uint(x), false, false); return fmaxf(__uint_as_float(s[0]), __uint_as_float(s[1])); }
__device__ __forceinline__ float red_q(float x) { x += dpp<ROR4>(x); x += dpp<XOR8>(x); return sw32_add(sw16_add(x)); }
__device__ __forceinline__ float red_all(float x) { x += dpp<XOR1>(x); x += dpp<XOR2>(x); return red_q(x); }
__device__ __forceinline__ float red4(float v0, float v1, float v2, float v3) {
  float u = tr32(tr16(v0, v1), tr16(v2, v3));
  u += dpp<ROR4>(u); u += dpp<XOR8>(u);
  return u;
}
__device__ __forceinline__ unsigned rotl(unsigned x, unsigned r) { return __builtin_amdgcn_alignbit(x, x, (32u - r) & 31u); }
__device__ __forceinline__ unsigned rec_fold(u32x4 w, unsigned r8) {
  unsigned h = rotl(w[0] ^ rotl(w[1], 2) ^ rotl(w[2], 4) ^ rotl(w[3], 6), r8);
  h ^= dppu<XOR1>(h); h ^= dppu<XOR2>(h);
  return h;
}
__device__ __forceinline__ u32x4 rec_seal(f32x4 v, unsigned tag, unsigned r8) {
  u32x4 w = {__float_as_uint(v[0]) & ~3u, __float_as_uint(v[1]) & ~3u, __float_as_uint(v[2]) & ~3u, __float_as_uint(v[3]) & ~3u};
  const unsigned cb = (rec_fold(w, r8) ^ tag) >> r8;
  w[0] |= cb & 3u; w[1] |= (cb >> 2) & 3u; w[2] |= (cb >> 4) & 3u; w[3] |= (cb >> 6) & 3u;
  return w;
}
__device__ __forceinline__ float rec_val(unsigned w) { return __uint_as_float(w & ~3u); }
__device__ __forceinline__ f32x4 proj_reduce(const f32x2 (&a)[4], int lane) {
  f32x4 r;
#pragma unroll
  for (int k = 0; k < 4; ++k) {
    const float lo = a[k >> 1][k & 1], hi = a[2 + (k >> 1)][k & 1];
    const float t1 = lo + dpp<XOR1>(lo), t2 = hi + dpp<XOR1>(hi);
    r[k] = sw32_add(sw16_add((lane & 1) ? t2 : t1));
  }
  return r;
}
__device__ __forceinline__ unsigned now() { return (unsigned)__builtin_amdgcn_s_memrealtime(); }
__device__ __forceinline__ unsigned lds_peek(unsigned byte_addr) {
  unsigned v;
  asm volatile("ds_read_b32 %0, %1\n\ts_waitcnt lgkmcnt(0)" : "=v"(v) : "v"(byte_addr) : "memory");
  return __builtin_amdgcn_readfirstlane(v);
}
__device__ __forceinline__ void lds_poke(unsigned byte_addr, unsigned v) { asm volatile("ds_write_b32 %0, %1" :: "v"(byte_addr), "v"(v) : "memory"); }
__device__ __forceinline__ void lds_wait(unsigned byte_addr, unsigned atleast) {
  for (unsigned spins = 0; lds_peek(byte_addr) < atleast && spins < SPIN_CAP; ++spins) __builtin_amdgcn_s_sleep(1);
}

__global__ void __launch_bounds__(NTHR, (NWAVE + 3) / 4)
k_fused(const unsigned char* __restrict__ Kc, const unsigned char* __restrict__ Vc, const unsigned short* __restrict__ x,
        const unsigned short* __restrict__ wq, const unsigned short* __restrict__ wo, const unsigned short* __restrict__ w1,
        const unsigned short* __restrict__ b1, const unsigned short* __restrict__ w2, const unsigned short* __restrict__ b2,
        const unsigned short* __restrict__ gw, unsigned short* __restrict__ out, unsigned char* ws) {
  __shared__ __attribute__((aligned(16))) unsigned lds[L_END];
  const unsigned lbase = (unsigned)(size_t)(LAS unsigned*)lds;
  const int b = blockIdx.x;
  const int tid = threadIdx.x, lane = tid & 63, wave = __builtin_amdgcn_readfirstlane(tid >> 6);

  if (__builtin_expect(wave == 0, 1)) {
    __builtin_amdgcn_s_setprio(3);
    const auto rk = RSRC(Kc, HKV * SK * DH * 2);
    const auto rv = RSRC(Vc, HKV * SK * DH * 2);
    const unsigned voff = lane * 16;
    unsigned off = (unsigned)b * (unsigned)(RANGE_TILES * TILE_BYTES), slot = 0;
    const unsigned fulla = lbase + L_FULL * 4, ctla = lbase + (L_FREE + (lane < NC ? lane : 0)) * 4;
    int issued = 0;
#define DMA(R, S, O) __builtin_amdgcn_raw_ptr_buffer_load_lds(R, (LAS void*)((LAS unsigned char*)lds + (S)), 16, voff, off, O, 2)
#define PUB() do { asm volatile("s_waitcnt vmcnt(" STR(WIN - GL) ")" ::: "memory"); lds_poke(fulla, max(lmax, issued - (WIN - GL))); issued += GL; } while (0)
#define WONLY() do { if (GL == 1) { asm volatile("s_waitcnt vmcnt(" STR(WIN - GL) ")" ::: "memory"); issued += 1; } } while (0)
#define PART(R, S) do { PUB(); DMA(R, S, 0); WONLY(); DMA(R, S, 1024); if (GL <= 2) PUB(); DMA(R, S, 2048); WONLY(); DMA(R, S, 3072); } while (0)
#define TILE(R) do { PART(R, slot); off += TILE_BYTES; slot += TILE_BYTES; } while (0)
    int lmax = 0;
    DMA(rk, 0, 0); DMA(rk, 0, 1024); DMA(rk, 0, 2048); DMA(rk, 0, 3072);
    off += TILE_BYTES;
    DMA(rk, TILE_BYTES, 0); DMA(rk, TILE_BYTES, 1024); DMA(rk, TILE_BYTES, 2048); DMA(rk, TILE_BYTES, 3072);
    off += TILE_BYTES; slot = 2 * TILE_BYTES; issued = 8;
    if (lane < 32) lds_poke(fulla + lane * 4, 0u);
    TILE(rk); TILE(rk);
    if (B0FIRST) TILE(rk);
    LGKM0();
    __builtin_amdgcn_s_barrier();
    if (!B0FIRST) TILE(rk);
    TILE(rk); TILE(rk); TILE(rk);
#define RINGWAIT(t) do { \
      if (slot == RT * TILE_BYTES) slot = 0; \
      const unsigned need = (t) + 8 > RT ? (unsigned)((t) + 8 - RT) : 0u; \
      unsigned v; \
      asm volatile("ds_read_b32 %0, %1\n\ts_waitcnt lgkmcnt(0)" : "=v"(v) : "v"(ctla) : "memory"); \
      if (__any(v < need)) { \
        asm volatile("s_waitcnt vmcnt(0)" ::: "memory"); \
        lmax = issued; lds_poke(fulla, issued); \
        unsigned spins = 0; \
        do { __builtin_amdgcn_s_sleep(1); asm volatile("ds_read_b32 %0, %1\n\ts_waitcnt lgkmcnt(0)" : "=v"(v) : "v"(ctla) : "memory"); } while (__any(v < need) && ++spins < SPIN_CAP); \
      } } while (0)
    for (int t = 8; t < RANGE_TILES; t += 8) {
      RINGWAIT(t);
      TILE(rk); TILE(rk); TILE(rk); TILE(rk); TILE(rk); TILE(rk); TILE(rk); TILE(rk);
    }
    off -= RANGE_TILES * TILE_BYTES;
    for (int t = RANGE_TILES; t < 2 * RANGE_TILES; t += 8) {
      RINGWAIT(t);
      TILE(rv); TILE(rv); TILE(rv); TILE(rv); TILE(rv); TILE(rv); TILE(rv); TILE(rv);
    }
#define TAILPUB(n) if (WIN > (n)) { asm volatile("s_waitcnt vmcnt(" #n ")" ::: "memory"); lds_poke(fulla, RANGE_TILES * 8 - (n)); }
    TAILPUB(36) TAILPUB(32) TAILPUB(28) TAILPUB(24) TAILPUB(20) TAILPUB(16) TAILPUB(12) TAILPUB(8) TAILPUB(4) TAILPUB(0)
    return;
  }

  const int g = b >> 5, j = b & 31;
  const unsigned r8 = 8 * (lane & 3);
  const auto rws = RSRC(ws, WS_END);

  if (wave >= 3) {
    const int sw = wave - 3, c = lane & 31, t = lane & 15;
    constexpr float C2 = 0.0625f * 1.4426950408889634f;
    __builtin_amdgcn_s_barrier();
    u32x4 q0, q1;
    float m = -INFINITY;
    float x_[8];
    unsigned full = 0, tag;
    float* scl = reinterpret_cast<float*>(&lds[L_SC + sw * 16 + (t >> 2) * 4 + (lane >> 5) * 2 + ((t >> 1) & 1)]);
#define WAITFULL(n) do { for (unsigned spins = 0; full < (unsigned)(n) && spins < SPIN_CAP; ++spins) { full = lds_peek(lbase + L_FULL * 4); if (full >= (unsigned)(n)) break; __builtin_amdgcn_s_sleep(1); } } while (0)
#define RD4(dst, tile) do { const u32x4* src_ = reinterpret_cast<const u32x4*>(&lds[((tile) % RT) * 1024 + lane * 4]); \
      CBAR(); dst[0] = src_[0]; dst[1] = src_[64]; dst[2] = src_[128]; dst[3] = src_[192]; } while (0)
#define PIN4(a) asm volatile("" : "+v"(a[0]), "+v"(a[1]), "+v"(a[2]), "+v"(a[3]) :: "memory")
#define RELEASE(n) do { LGKM0(); lds_poke(lbase + (L_FREE + sw) * 4, (n)); } while (0)
#define QK1(KK, i) { float a_ = 0.f, b_ = 0.f; \
      _Pragma("unroll") for (int k = 0; k < 4; ++k) { a_ = dot2((KK)[k], q0[k], a_); b_ = dot2((KK)[k], q1[k], b_); } \
      x_[2 * (i)] = a_; x_[2 * (i) + 1] = b_; }
#define TR_STEP(HALF, CTRL, BIT) _Pragma("unroll") for (int h = 0; h < (HALF); ++h) { \
      const float lo_ = x_[h], hi_ = x_[h + (HALF)]; const float t1_ = lo_ + dpp<CTRL>(lo_), t2_ = hi_ + dpp<CTRL>(hi_); \
      x_[h] = (t & (BIT)) ? t2_ : t1_; }
#define SCORES(KB, n) { QK1(KB[0], 0) QK1(KB[1], 1) QK1(KB[2], 2) QK1(KB[3], 3) \
      TR_STEP(4, XOR8, 8) TR_STEP(2, XOR7, 4) TR_STEP(1, XOR2, 2) x_[0] += dpp<XOR1>(x_[0]); \
      const float sc = sw16_add(x_[0]) * C2; \
      m = fmaxf(m, sc); \
      if ((lane & 17) == 0) scl[(n) * NC * 16] = sc; }
#define ALLFLAGS(base) do { for (unsigned spins = 0; spins < SPIN_CAP; ++spins) { unsigned fl; \
      asm volatile("ds_read_b32 %0, %1\n\ts_waitcnt lgkmcnt(0)" : "=v"(fl) : "v"(lbase + ((base) + (lane & (NC - 1))) * 4) : "memory"); \
      if (__all(fl != 0u)) break; __builtin_amdgcn_s_sleep(1); } } while (0)
    {
      {
        u32x4 Kb[HD][4];
#pragma unroll
        for (int d = 0; d < HD; ++d) {
          const int tile = d * NC + sw;
          WAITFULL(tile * 4 + 4);
          RD4(Kb[d], tile);
          RELEASE(tile + NC);
          PIN4(Kb[d]);
        }
        lds_wait(lbase + L_QRDY * 4, 1u);
        q0 = *reinterpret_cast<const u32x4*>(&lds[L_Q + 4 * c]); q1 = *reinterpret_cast<const u32x4*>(&lds[L_Q + 128 + 4 * c]);
        tag = lds_peek(lbase + L_TAG * 4);
#pragma unroll
        for (int d = 0; d < HD; ++d) { SCORES(Kb[d], d) SB(); }
      }
      for (int n = HD; n < RANGE_TILES / NC; ++n) {
        const int tile = n * NC + sw;
        u32x4 Kb[4];
        WAITFULL(tile * 4 + 4);
        RD4(Kb, tile);
        RELEASE(tile + NC > RANGE_TILES ? RANGE_TILES : tile + NC);
        PIN4(Kb);
        SCORES(Kb, n)
        SB();
      }
      m = fmaxf(m, dpp<ROR4>(m)); m = fmaxf(m, dpp<XOR8>(m)); m = sw32_max(m);
      {
        float* mx = reinterpret_cast<float*>(&lds[L_MX]);
        if (lane == 0 || lane == 2) mx[sw * 2 + (lane >> 1)] = m;
        LGKM0();
        lds_poke(lbase + (L_MF + sw) * 4, 1u);
        ALLFLAGS(L_MF);
        float M0 = mx[0], M1 = mx[1];
#pragma unroll
        for (int w = 1; w < NC; ++w) { M0 = fmaxf(M0, mx[2 * w]); M1 = fmaxf(M1, mx[2 * w + 1]); }
        f32x4* ps = reinterpret_cast<f32x4*>(&lds[L_SC + sw * 1024 + lane * 4]);
        float l0 = 0.f, l1 = 0.f;
#pragma unroll
        for (int i = 0; i < 4; ++i) {
          f32x4 sv = ps[i * 64];
          sv[0] = __builtin_amdgcn_exp2f(sv[0] - M0); sv[1] = __builtin_amdgcn_exp2f(sv[1] - M1);
          sv[2] = __builtin_amdgcn_exp2f(sv[2] - M0); sv[3] = __builtin_amdgcn_exp2f(sv[3] - M1);
          l0 += sv[0] + sv[2]; l1 += sv[1] + sv[3];
          ps[i * 64] = sv;
        }
        l0 = red_all(l0); l1 = red_all(l1);
        if (lane == 0) *reinterpret_cast<f32x4*>(&lds[L_LS + sw * 4]) = f32x4{l0, l1, M0, M1};
        LGKM0();
        lds_poke(lbase + (L_PF + sw) * 4, 1u);
        ALLFLAGS(L_PF);
      }
      f32x2 acc0[4], acc1[4];
#pragma unroll
      for (int k = 0; k < 4; ++k) { acc0[k] = f32x2{0.f, 0.f}; acc1[k] = f32x2{0.f, 0.f}; }
      const unsigned* vsrc = &lds[(lane >> 3) * 128 + sw * 32 + (lane & 7) * 4];
      const float* psrc = reinterpret_cast<const float*>(&lds[L_SC + (lane >> 3) * 2]);
#define PVT(VV, PP) { _Pragma("unroll") for (int k = 0; k < 4; ++k) { const f32x2 vf = {bflo((VV)[k]), bfhi((VV)[k])}; \
        acc0[k] = __builtin_elementwise_fma(f32x2{(PP)[0], (PP)[0]}, vf, acc0[k]); acc1[k] = __builtin_elementwise_fma(f32x2{(PP)[1], (PP)[1]}, vf, acc1[k]); } }
#define PINACC() _Pragma("unroll") for (int k = 0; k < 4; ++k) { asm volatile("" : "+v"(acc0[k])); asm volatile("" : "+v"(acc1[k])); }
      for (int n = 0; n < RANGE_TILES - 4; n += 4) {
        u32x4 Vb[4]; f32x2 pp[4];
#pragma unroll
        for (int i = 0; i < 4; ++i) pp[i] = *reinterpret_cast<const f32x2*>(psrc + (n + i) * 16);
        WAITFULL((RANGE_TILES + n + 4) * 4);
        CBAR();
#pragma unroll
        for (int i = 0; i < 4; ++i) Vb[i] = *reinterpret_cast<const u32x4*>(vsrc + ((n + i) % RT) * 1024);
        RELEASE(RANGE_TILES + n + 4);
        PIN4(Vb);
        PVT(Vb[0], pp[0]) PVT(Vb[1], pp[1]) PVT(Vb[2], pp[2]) PVT(Vb[3], pp[3])
        PINACC(); SB();
      }
      lds_wait(lbase + L_WRDY * 4, 1u);
      const float wsc = reinterpret_cast<const float*>(&lds[L_WS])[lane >> 5];
#pragma unroll
      for (int i = 0; i < 4; ++i) {
        const int n = RANGE_TILES - 4 + i;
        const f32x2 pp = *reinterpret_cast<const f32x2*>(psrc + n * 16);
        WAITFULL((RANGE_TILES + n + 1) * 4);
        CBAR();
        const u32x4 Vb = *reinterpret_cast<const u32x4*>(vsrc + (n % RT) * 1024);
        PVT(Vb, pp)
        PINACC(); SB();
      }
      {
        float u[8];
#pragma unroll
        for (int k = 0; k < 4; ++k) { u[2 * k] = tr32(acc0[k][0], acc1[k][0]); u[2 * k + 1] = tr32(acc0[k][1], acc1[k][1]); }
        f32x4 r;
#pragma unroll
        for (int k = 0; k < 4; ++k) { const float v_ = tr16(u[k], u[k + 4]); r[k] = (v_ + dpp<XOR8>(v_)) * wsc; }
        const int jc = (lane >> 5) * 16 + (2 * sw + ((lane >> 2) & 1)) * 2 + ((lane >> 4) & 1);
        const u32x4 rec = rec_seal(r, tag, r8);
        if ((lane & 8) == 0) ST128(rec, rws, WS_P1 + ((g * NJ + jc) * NJ + j) * 64 + (lane & 3) * 16);
      }
      if (b < NRED && sw < 4) {
        u32x4 v[4], w_[4]; unsigned spins = 0u;
        const unsigned ybase = WS_Y + (b * NWG + sw * 64) * 64 + lane * 16;
        lds_wait(lbase + L_H1 * 4, 1u);
#define YLOAD(dst) do { CBAR(); _Pragma("unroll") for (int i = 0; i < 4; ++i) dst[i] = LD128(rws, ybase + i * 1024, 0, 16); CBAR(); } while (0)
#define YOK(src) __all((((rec_fold(src[0], r8) ^ tag) | (rec_fold(src[1], r8) ^ tag)) | ((rec_fold(src[2], r8) ^ tag) | (rec_fold(src[3], r8) ^ tag))) == 0u)
        YLOAD(v);
        __builtin_amdgcn_s_sleep(PIPE);
        for (;;) {
          YLOAD(w_);
          if (YOK(v)) break;
          YLOAD(v);
          if (YOK(w_) || ++spins > SPIN_CAP) {
#pragma unroll
            for (int i = 0; i < 4; ++i) v[i] = w_[i];
            break;
          }
        }
        float s[4];
#pragma unroll
        for (int k = 0; k < 4; ++k) s[k] = (rec_val(v[0][k]) + rec_val(v[1][k])) + (rec_val(v[2][k]) + rec_val(v[3][k]));
        const float u = red4(s[0], s[1], s[2], s[3]);
        float* fin = reinterpret_cast<float*>(&lds[L_FINV]);
        if ((lane & 12) == 0) fin[sw * 16 + (lane & 3) * 4 + (lane >> 4)] = u;
        unsigned old = 0;
        CBAR();
        if (lane == 0) old = __hip_atomic_fetch_add((LAS unsigned*)&lds[L_FIN], 1u, RLX_WG);
        old = __builtin_amdgcn_readfirstlane(old);
        CBAR();
        if (old == 3u) {
          if (lane < 16) out[b * 16 + lane] = (unsigned short)f2bf((fin[lane] + fin[16 + lane]) + (fin[32 + lane] + fin[48 + lane]));
          if (b == 0 && lane == 0) __builtin_amdgcn_raw_buffer_store_b64(u32x2{lds[L_TAG + 1], lds[L_TAG + 2]}, rws, WS_EPOCH, 0, 16);
        }
      }
    }
    return;
  }

  const int cw = wave - 1;
  if (B0FIRST == 1) __builtin_amdgcn_s_barrier();
  const unsigned tc0 = now();
  const int rr = (lane & 1) | ((lane >> 4) << 1);
  unsigned tag;
  u32x2 wov[16];
#define WO_LOAD() do { const auto ro = RSRC(wo, HQ * DH * DM * 2); const int d0 = g * 512 + (j >> 1) * 32 + (j & 1) * 4; \
    _Pragma("unroll") for (int r = 0; r < 16; ++r) wov[r] = __builtin_amdgcn_raw_buffer_load_b64(ro, (d0 + (r >> 2) * 8 + (r & 3)) * (DM * 2) + cw * 448 + lane * 8, 0, 2); } while (0)
  if (cw == 0) {
    const int qcg = b >> 2, qrg = b & 3, row0 = qrg * 112 + rr * 14;
    const auto rx = RSRC(x, DM * 2);
    const auto rq = RSRC(wq, DM * HQ * DH * 2);
    u32x4 w[14];
#pragma unroll
    for (int i = 0; i < 14; ++i) w[i] = LD128(rq, row0 * (HQ * DH * 2) + qcg * 128 + (lane & 14) * 8, i * (HQ * DH * 2), 2);
    const u32x4 xa = LD128(rx, row0 * 2, 0, 0);
    const u32x3 xb = __builtin_amdgcn_raw_buffer_load_b96(rx, row0 * 2 + 16, 0, 0);
    const u32x2 ep = __builtin_amdgcn_raw_buffer_load_b64(rws, WS_EPOCH, 0, 16);
    SB();
    if (B0FIRST == 2) { __builtin_amdgcn_s_barrier(); SB(); }
    if (EARLYW) WO_LOAD();
    SB();
    if (!B0FIRST) __builtin_amdgcn_s_barrier();
    SB();
    const unsigned xw[7] = {xa[0], xa[1], xa[2], xa[3], xb[0], xb[1], xb[2]};
    f32x2 a[4];
#pragma unroll
    for (int k = 0; k < 4; ++k) a[k] = f32x2{0.f, 0.f};
#pragma unroll
    for (int i = 0; i < 14; ++i) {
      const float xv = (i & 1) ? bfhi(xw[i >> 1]) : bflo(xw[i >> 1]);
#pragma unroll
      for (int k = 0; k < 4; ++k) a[k] = __builtin_elementwise_fma(f32x2{xv, xv}, f32x2{bflo(w[i][k]), bfhi(w[i][k])}, a[k]);
    }
    const unsigned long long tg = (((unsigned long long)ep[1] << 32) | ep[0]) + 1ull;
    tag = 2u * (unsigned)tg + 1u;
    {
      const u32x4 rec = rec_seal(proj_reduce(a, lane), tag, r8);
      if (lane < 16) ST128(rec, rws, WS_QG + (qrg * 4096 + qcg * 64) * 4 + lane * 16);
    }
    u32x4 gq[8]; unsigned go[8], spins = 0;
#pragma unroll
    for (int i = 0; i < 8; ++i) { go[i] = WS_QG + ((i >> 1) * 4096 + g * 512 + (i & 1) * 256) * 4 + lane * 16; gq[i] = u32x4{0u, 0u, 0u, 0u}; }
    while (now() - tc0 < QPOLL_T) __builtin_amdgcn_s_sleep(2);
    for (;;) {
      CBAR();
      u32x4 t_[8];
#pragma unroll
      for (int i = 0; i < 8; ++i) t_[i] = LD128(rws, go[i], 0, 16);
      bool all = true;
#pragma unroll
      for (int i = 0; i < 8; ++i) {
        if (rec_fold(t_[i], r8) == tag) { gq[i] = t_[i]; go[i] = OOB; }
        all &= go[i] == OOB;
      }
      if (__all(all) || ++spins > SPIN_CAP) break;
      __builtin_amdgcn_s_sleep(8);
    }
#pragma unroll
    for (int hf = 0; hf < 2; ++hf) {
      float qs[4];
#pragma unroll
      for (int k = 0; k < 4; ++k) qs[k] = (rec_val(gq[hf][k]) + rec_val(gq[2 + hf][k])) + (rec_val(gq[4 + hf][k]) + rec_val(gq[6 + hf][k]));
      *reinterpret_cast<u32x2*>(&lds[L_Q + hf * 128 + 2 * lane]) = u32x2{f2bf(qs[0]) | (f2bf(qs[1]) << 16), f2bf(qs[2]) | (f2bf(qs[3]) << 16)};
    }
    if (lane == 0) { lds[L_TAG] = tag; lds[L_TAG + 1] = (unsigned)tg; lds[L_TAG + 2] = (unsigned)(tg >> 32); }
    LGKM0();
    lds_poke(lbase + L_QRDY * 4, 1u);
    if (!EARLYW) WO_LOAD();
  } else {
    if (B0FIRST == 2) __builtin_amdgcn_s_barrier();
    if (!EARLYW) { if (!B0FIRST) __builtin_amdgcn_s_barrier(); lds_wait(lbase + L_QRDY * 4, 1u); }
    const bool mlp = b < 224;
    const auto rx = RSRC(x, DM * 2);
    const auto rg = RSRC(gw, DM * 2);
    const auto rb2 = RSRC(b2, DM * 2);
    const auto r1 = RSRC(w1, mlp ? DM * FF * 2 : 0);
    const auto r2 = RSRC(w2, mlp ? FF * DM * 2 : 0);
    const auto rb = RSRC(b1, FF * 2);
    const int row0 = (b & 7) * 56 + rr * 7, hc0 = (mlp ? b : 0) * 8;
    u32x4 w1v[7], wb[8]; unsigned short xr[7];
#pragma unroll
    for (int i = 0; i < 7; ++i) { w1v[i] = LD128(r1, (row0 + i) * (FF * 2) + (b >> 3) * 128 + (lane & 14) * 8, 0, 2); xr[i] = x[row0 + i]; }
    const u32x4 xv = LD128(rx, lane * 16, 0, 0), gv = LD128(rg, lane * 16, 0, 0), b2v = LD128(rb2, lane * 16, 0, 0);
    const u32x4 bv = LD128(rb, hc0 * 2, 0, 0);
#pragma unroll
    for (int i = 0; i < 8; ++i) wb[i] = LD128(r2, (hc0 + i) * (DM * 2) + lane * 16, 0, 2);
    WO_LOAD();
    SB();
    if (EARLYW) { if (!B0FIRST) __builtin_amdgcn_s_barrier(); lds_wait(lbase + L_QRDY * 4, 1u); }
    tag = lds_peek(lbase + L_TAG * 4);
    {
      f32x2 hv[4];
#pragma unroll
      for (int k = 0; k < 4; ++k) hv[k] = f32x2{0.f, 0.f};
#pragma unroll
      for (int i = 0; i < 7; ++i) {
        const float xf = bflo(xr[i]);
#pragma unroll
        for (int k = 0; k < 4; ++k) hv[k] = __builtin_elementwise_fma(f32x2{xf, xf}, f32x2{bflo(w1v[i][k]), bfhi(w1v[i][k])}, hv[k]);
      }
      const u32x4 rec = rec_seal(proj_reduce(hv, lane), tag, r8);
      if (mlp && lane < 16) ST128(rec, rws, WS_HP + ((b & 7) * FF + (b >> 3) * 64) * 4 + lane * 16);
    }
    float s = 0.f;
#pragma unroll
    for (int k = 0; k < 4; ++k) { s = fmaf(bflo(xv[k]), bflo(gv[k]), s); s = fmaf(bfhi(xv[k]), bfhi(gv[k]), s); }
    s = red_all(s);
    const float gate = 1.f / (1.f + __expf(-s));
    const float bsc = b == NWG - 1 ? 1.f - gate : 0.f;
    f32x2 y2[4];
#pragma unroll
    for (int k = 0; k < 4; ++k) y2[k] = f32x2{bsc * bflo(b2v[k]), bsc * bfhi(b2v[k])};
    if (mlp) {
      u32x4 hr; unsigned spins = 0;
      while (now() - tc0 < HPOLL_T) __builtin_amdgcn_s_sleep(16);
      for (;;) {
        CBAR();
        hr = LD128(rws, WS_HP + (((lane >> 2) & 7) * FF + (b >> 1) * 16) * 4 + (lane & 3) * 16, 0, 16);
        if (__all(rec_fold(hr, r8) == tag) || ++spins > SPIN_CAP) break;
        __builtin_amdgcn_s_sleep(16);
      }
      float hs[4];
#pragma unroll
      for (int k = 0; k < 4; ++k) { float v_ = rec_val(hr[k]); v_ += dpp<ROR4>(v_); v_ += dpp<XOR8>(v_); hs[k] = sw16_add(v_); }
#pragma unroll
      for (int k = 0; k < 8; ++k) {
        const float h = __uint_as_float(__builtin_amdgcn_readlane(__float_as_uint(hs[k & 3]), (b & 1) * 2 + (k >> 2))) + ((k & 1) ? bfhi(bv[k >> 1]) : bflo(bv[k >> 1]));
        const float u = 0.7978845608028654f * (h + 0.044715f * h * h * h);
        const float ge = 0.5f * h * (1.f + tanhf(u)) * (1.f - gate);
#pragma unroll
        for (int k2 = 0; k2 < 4; ++k2) y2[k2] = __builtin_elementwise_fma(f32x2{ge, ge}, f32x2{bflo(wb[k][k2]), bfhi(wb[k][k2])}, y2[k2]);
      }
    }
    float* yl = reinterpret_cast<float*>(&lds[L_Y2]);
    *reinterpret_cast<f32x4*>(yl + 8 * lane) = f32x4{y2[0][0], y2[0][1], y2[1][0], y2[1][1]};
    *reinterpret_cast<f32x4*>(yl + 8 * lane + 4) = f32x4{y2[2][0], y2[2][1], y2[3][0], y2[3][1]};
    if (lane == 0) yl[512] = gate;
    LGKM0();
    lds_poke(lbase + L_YRDY * 4, 1u);
  }
  f32x2 wof[16][2];
#pragma unroll
  for (int r = 0; r < 16; ++r)
#pragma unroll
    for (int k = 0; k < 2; ++k) { wof[r][k] = f32x2{bflo(wov[r][k]), bfhi(wov[r][k])}; asm volatile("" : "+v"(wof[r][k])); }
  lds_wait(lbase + L_YRDY * 4, 1u);
  const float* yl = reinterpret_cast<const float*>(&lds[L_Y2]);
  const f32x4 ya = *reinterpret_cast<const f32x4*>(yl + 224 * cw + 4 * lane);
  if (cw == 0) {
    const float gate = yl[512];
    for (unsigned spins = 0; spins < SPIN_CAP; ++spins) {
      unsigned fl;
      asm volatile("ds_read_b32 %0, %1\n\ts_waitcnt lgkmcnt(0)" : "=v"(fl) : "v"(lbase + (L_PF + (lane & (NC - 1))) * 4) : "memory");
      if (__all(fl != 0u)) break;
      __builtin_amdgcn_s_sleep(8);
    }
    const float* ls = reinterpret_cast<const float*>(&lds[L_LS]);
    const int h = lane >> 5;
    const float mo = ls[2 + h], lo_ = (ls[h] + ls[4 + h]) + (ls[8 + h] + ls[12 + h]);
    if ((lane & 31) == 0) ST128((u32x4{__float_as_uint(mo), __float_as_uint(lo_), __float_as_uint(mo) ^ rotl(__float_as_uint(lo_), 16) ^ tag, 0u}), rws, WS_ML + ((g * 2 + h) * NJ + j) * 16);
    u32x4 ml;
    const unsigned tw = now();
    while (now() - tw < MLPOLL_T) __builtin_amdgcn_s_sleep(16);
    for (unsigned spins = 0;; ++spins) {
      CBAR();
      ml = LD128(rws, WS_ML + (g * 2 * NJ + lane) * 16, 0, 16);
      if (__all((ml[0] ^ rotl(ml[1], 16) ^ ml[2]) == tag) || spins > SPIN_CAP) break;
      __builtin_amdgcn_s_sleep(32);
    }
    const float ms = __uint_as_float(ml[0]);
    float M = ms;
    M = fmaxf(M, dpp<XOR1>(M)); M = fmaxf(M, dpp<XOR2>(M)); M = fmaxf(M, dpp<ROR4>(M)); M = fmaxf(M, dpp<XOR8>(M)); M = sw16_max(M);
    float L = __builtin_amdgcn_exp2f(ms - M) * __uint_as_float(ml[1]);
    L += dpp<XOR1>(L); L += dpp<XOR2>(L); L += dpp<ROR4>(L); L += dpp<XOR8>(L); L = sw16_add(L);
    if ((lane & 31) == 0) reinterpret_cast<float*>(&lds[L_WS])[h] = gate * __builtin_amdgcn_exp2f(mo - M) / L;
    LGKM0();
    lds_poke(lbase + L_WRDY * 4, 1u);
  }
  {
    for (unsigned spins = 0; lds_peek(lbase + L_FULL * 4) < (unsigned)(RANGE_TILES * 8) && spins < SPIN_CAP; ++spins) __builtin_amdgcn_s_sleep(4);
    u32x4 pa, pb, pa2, pb2; unsigned spins = 0u;
    const unsigned pbase = WS_P1 + (g * NJ + j) * NJ * 64 + lane * 16;
#define HLOAD(A, B) do { CBAR(); A = LD128(rws, pbase, 0, 16); B = LD128(rws, pbase + 1024, 0, 16); CBAR(); } while (0)
#define HBAD(A, B) __builtin_amdgcn_ballot_w64(((rec_fold(A, r8) ^ tag) | (rec_fold(B, r8) ^ tag)) != 0u)
    unsigned long long bad;
    for (;;) {
      HLOAD(pa, pb);
      bad = HBAD(pa, pb);
      if (__builtin_popcountll(bad) <= LAZYN || ++spins > SPIN_CAP) break;
      __builtin_amdgcn_s_sleep(LAZY);
    }
    if (bad) {
      HLOAD(pa, pb);
      __builtin_amdgcn_s_sleep(PIPE);
      for (;;) {
        HLOAD(pa2, pb2);
        if (!HBAD(pa, pb)) break;
        HLOAD(pa, pb);
        if (!HBAD(pa2, pb2) || ++spins > SPIN_CAP) { pa = pa2; pb = pb2; break; }
      }
    }
    const float u = red4(rec_val(pa[0]) + rec_val(pb[0]), rec_val(pa[1]) + rec_val(pb[1]), rec_val(pa[2]) + rec_val(pb[2]), rec_val(pa[3]) + rec_val(pb[3]));
    f32x2 y[2], z[2];
#define OROW(r) __uint_as_float(__builtin_amdgcn_readlane(__float_as_uint(u), 16 * ((r) & 3) + ((r) >> 2)))
    { const float o0 = OROW(0), o1 = OROW(1);
      y[0] = __builtin_elementwise_fma(f32x2{o0, o0}, wof[0][0], f32x2{ya[0], ya[1]}); y[1] = __builtin_elementwise_fma(f32x2{o0, o0}, wof[0][1], f32x2{ya[2], ya[3]});
      z[0] = o1 * wof[1][0]; z[1] = o1 * wof[1][1]; }
#define YR2(r) { const float o0 = OROW(r), o1 = OROW((r) + 1); \
      y[0] = __builtin_elementwise_fma(f32x2{o0, o0}, wof[r][0], y[0]); y[1] = __builtin_elementwise_fma(f32x2{o0, o0}, wof[r][1], y[1]); \
      z[0] = __builtin_elementwise_fma(f32x2{o1, o1}, wof[(r) + 1][0], z[0]); z[1] = __builtin_elementwise_fma(f32x2{o1, o1}, wof[(r) + 1][1], z[1]); }
    YR2(2) YR2(4) YR2(6) YR2(8) YR2(10) YR2(12) YR2(14)
    y[0] += z[0]; y[1] += z[1];
    const u32x4 yv = rec_seal(f32x4{y[0][0], y[0][1], y[1][0], y[1][1]}, tag, r8);
    if (lane < 56) ST128(yv, rws, WS_Y + (((cw * 14 + (lane >> 2)) * NWG + b) * 64 + (lane & 3) * 16));
    if (cw == 0) { lds_poke(lbase + L_H1 * 4, 1u); }
  }
}

extern "C" void kernel_launch(void* const* d_in, const int* in_sizes, int n_in, void* d_out, int out_size, void* d_ws, size_t ws_size, hipStream_t stream) {
  k_fused<<<NWG, NTHR, 0, stream>>>((const unsigned char*)d_in[1], (const unsigned char*)d_in[2], (const unsigned short*)d_in[0], (const unsigned short*)d_in[3],
                                    (const unsigned short*)d_in[4], (const unsigned short*)d_in[6], (const unsigned short*)d_in[7], (const unsigned short*)d_in[8],
                                    (const unsigned short*)d_in[9], (const unsigned short*)d_in[10], (unsigned short*)d_out, (unsigned char*)d_ws);
}
```

```cpp
#include <hip/hip_runtime.h>
#include <hip/hip_bf16.h>
#include <cstdint>

#define NC 4
#define HD 12
#define RT 32
#define GL 4
#define WIN 20
#define LAZY 12
#define LAZYN 16
#define PIPE 5
#define HPOLL_T 1500
#define QPOLL_T 550
#define MLPOLL_T 500

constexpr int DM = 448, HQ = 16, HKV = 8, SK = 65536, DH = 256, FF = 1792;
constexpr int NWG = 256, NJ = 32, NWAVE = NC + 3, NTHR = NWAVE * 64;
constexpr int TILE_BYTES = 4096;
constexpr int RANGE_TILES = SK / 8 / NJ;
static_assert(NC == 4 && RT % 8 == 0 && RANGE_TILES % NC == 0 && HD >= 1 && HD <= 12 && (GL == 1 || GL == 2 || GL == 4) && WIN % GL == 0 && WIN <= 40, "deal");
constexpr int NRED = 28;
constexpr unsigned SPIN_CAP = 1u << 20, OOB = 0x80000000u;

constexpr unsigned WS_EPOCH = 0, WS_QG = 8192, WS_HP = WS_QG + 4 * 4096 * 4, WS_ML = WS_HP + 8 * FF * 4, WS_P1 = WS_ML + HQ * NJ * 16,
                   WS_Y = WS_P1 + HKV * NJ * NJ * 64, WS_END = WS_Y + NRED * NWG * 64;
constexpr int L_SC = RT * 1024, L_Q = L_SC + RANGE_TILES * 16, L_FULL = L_Q + 256, L_QRDY = L_FULL + 1, L_FIN = L_FULL + 2, L_YRDY = L_FULL + 3, L_FREE = L_FULL + 4,
              L_PF = L_FULL + 12, L_H1 = L_FULL + 20, L_WRDY = L_FULL + 21, L_MF = L_FULL + 24, L_TAG = L_FULL + 32, L_MX = L_FULL + 36, L_LS = L_FULL + 52,
              L_WS = L_FULL + 68, L_FINV = L_FULL + 72, L_Y2 = L_FINV + 64, L_END = L_Y2 + 520;
static_assert(L_END * 4 <= 160 * 1024, "LDS");

using bf16x2 = __attribute__((ext_vector_type(2))) __bf16;
using u32x2  = __attribute__((ext_vector_type(2))) unsigned;
using u32x3  = __attribute__((ext_vector_type(3))) unsigned;
using u32x4  = __attribute__((ext_vector_type(4))) unsigned;
using f32x2  = __attribute__((ext_vector_type(2))) float;
using f32x4  = __attribute__((ext_vector_type(4))) float;
#define LAS __attribute__((address_space(3)))
#define RLX_WG __ATOMIC_RELAXED, __HIP_MEMORY_SCOPE_WORKGROUP
#define CBAR() asm volatile("" ::: "memory")
#define SB() __builtin_amdgcn_sched_barrier(0)
#define LGKM0() asm volatile("s_waitcnt lgkmcnt(0)" ::: "memory")
#define STR2(x) #x
#define STR(x) STR2(x)
#define RSRC(p, bytes) __builtin_amdgcn_make_buffer_rsrc((void*)(p), 0, (int)(bytes), 0x00020000)
#define LD128(r, off, soff, aux) __builtin_amdgcn_raw_buffer_load_b128(r, off, soff, aux)
#define ST128(v, r, off) __builtin_amdgcn_raw_buffer_store_b128(v, r, off, 0, 16)

__device__ __forceinline__ float dot2(unsigned k, unsigned q, float acc) {
  return __builtin_amdgcn_fdot2_f32_bf16(__builtin_bit_cast(bf16x2, k), __builtin_bit_cast(bf16x2, q), acc, false);
}
template <int CTRL> __device__ __forceinline__ float dpp(float x) {
  return __builtin_bit_cast(float, __builtin_amdgcn_mov_dpp(__builtin_bit_cast(int, x), CTRL, 0xf, 0xf, true));
}
template <int CTRL> __device__ __forceinline__ unsigned dppu(unsigned x) { return (unsigned)__builtin_amdgcn_mov_dpp((int)x, CTRL, 0xf, 0xf, true); }
constexpr int XOR1 = 0xB1, XOR2 = 0x4E, XOR7 = 0x141, XOR8 = 0x128, ROR4 = 0x124;
__device__ __forceinline__ float bflo(unsigned u) { return __builtin_bit_cast(float, u << 16); }
__device__ __forceinline__ float bfhi(unsigned u) { return __builtin_bit_cast(float, u & 0xffff0000u); }
__device__ __forceinline__ unsigned f2bf(float f) { __hip_bfloat16 b = __float2bfloat16(f); return __builtin_bit_cast(unsigned short, b); }
__device__ __forceinline__ float tr16(float a, float b) { auto s = __builtin_amdgcn_permlane16_swap(__float_as_uint(a), __float_as_uint(b), false, false); return __uint_as_float(s[0]) + __uint_as_float(s[1]); }
__device__ __forceinline__ float tr32(float a, float b) { auto s = __builtin_amdgcn_permlane32_swap(__float_as_uint(a), __float_as_uint(b), false, false); return __uint_as_float(s[0]) + __uint_as_float(s[1]); }
__device__ __forceinline__ float sw16_add(float x) { return tr16(x, x); }
__device__ __forceinline__ float sw32_add(float x) { return tr32(x, x); }
__device__ __forceinline__ float sw16_max(float x) { auto s = __builtin_amdgcn_permlane16_swap(__float_as_uint(x), __float_as_uint(x), false, false); return fmaxf(__uint_as_float(s[0]), __uint_as_float(s[1])); }
__device__ __forceinline__ float sw32_max(float x) { auto s = __builtin_amdgcn_permlane32_swap(__float_as_uint(x), __float_as_uint(x), false, false); return fmaxf(__uint_as_float(s[0]), __uint_as_float(s[1])); }
__device__ __forceinline__ float red_q(float x) { x += dpp<ROR4>(x); x += dpp<XOR8>(x); return sw32_add(sw16_add(x)); }
__device__ __forceinline__ float red_all(float x) { x += dpp<XOR1>(x); x += dpp<XOR2>(x); return red_q(x); }
__device__ __forceinline__ float red4(float v0, float v1, float v2, float v3) {
  float u = tr32(tr16(v0, v1), tr16(v2, v3));
  u += dpp<ROR4>(u); u += dpp<XOR8>(u);
  return u;
}
__device__ __forceinline__ unsigned rotl(unsigned x, unsigned r) { return __builtin_amdgcn_alignbit(x, x, (32u - r) & 31u); }
__device__ __forceinline__ unsigned rec_fold(u32x4 w, unsigned r8) {
  unsigned h = rotl(w[0] ^ rotl(w[1], 2) ^ rotl(w[2], 4) ^ rotl(w[3], 6), r8);
  h ^= dppu<XOR1>(h); h ^= dppu<XOR2>(h);
  return h;
}
__device__ __forceinline__ u32x4 rec_seal(f32x4 v, unsigned tag, unsigned r8) {
  u32x4 w = {__float_as_uint(v[0]) & ~3u, __float_as_uint(v[1]) & ~3u, __float_as_uint(v[2]) & ~3u, __float_as_uint(v[3]) & ~3u};
  const unsigned cb = (rec_fold(w, r8) ^ tag) >> r8;
  w[0] |= cb & 3u; w[1] |= (cb >> 2) & 3u; w[2] |= (cb >> 4) & 3u; w[3] |= (cb >> 6) & 3u;
  return w;
}
__device__ __forceinline__ float rec_val(unsigned w) { return __uint_as_float(w & ~3u); }
__device__ __forceinline__ f32x4 proj_reduce(const f32x2 (&a)[4], int lane) {
  f32x4 r;
#pragma unroll
  for (int k = 0; k < 4; ++k) {
    const float lo = a[k >> 1][k & 1], hi = a[2 + (k >> 1)][k & 1];
    const float t1 = lo + dpp<XOR1>(lo), t2 = hi + dpp<XOR1>(hi);
    r[k] = sw32_add(sw16_add((lane & 1) ? t2 : t1));
  }
  return r;
}
__device__ __forceinline__ unsigned now() { return (unsigned)__builtin_amdgcn_s_memrealtime(); }
__device__ __forceinline__ unsigned lds_peek(unsigned byte_addr) {
  unsigned v;
  asm volatile("ds_read_b32 %0, %1\n\ts_waitcnt lgkmcnt(0)" : "=v"(v) : "v"(byte_addr) : "memory");
  return __builtin_amdgcn_readfirstlane(v);
}
__device__ __forceinline__ void lds_poke(unsigned byte_addr, unsigned v) { asm volatile("ds_write_b32 %0, %1" :: "v"(byte_addr), "v"(v) : "memory"); }
__device__ __forceinline__ void lds_wait(unsigned byte_addr, unsigned atleast) {
  for (unsigned spins = 0; lds_peek(byte_addr) < atleast && spins < SPIN_CAP; ++spins) __builtin_amdgcn_s_sleep(1);
}

__global__ void __launch_bounds__(NTHR, (NWAVE + 3) / 4)
k_fused(const unsigned char* __restrict__ Kc, const unsigned char* __restrict__ Vc, const unsigned short* __restrict__ x,
        const unsigned short* __restrict__ wq, const unsigned short* __restrict__ wo, const unsigned short* __restrict__ w1,
        const unsigned short* __restrict__ b1, const unsigned short* __restrict__ w2, const unsigned short* __restrict__ b2,
        const unsigned short* __restrict__ gw, unsigned short* __restrict__ out, unsigned char* ws) {
  __shared__ __attribute__((aligned(16))) unsigned lds[L_END];
  const unsigned lbase = (unsigned)(size_t)(LAS unsigned*)lds;
  const int b = blockIdx.x;
  const int tid = threadIdx.x, lane = tid & 63, wave = __builtin_amdgcn_readfirstlane(tid >> 6);

  if (__builtin_expect(wave == 0, 1)) {
    __builtin_amdgcn_s_setprio(3);
    const auto rk = RSRC(Kc, HKV * SK * DH * 2);
    const auto rv = RSRC(Vc, HKV * SK * DH * 2);
    const unsigned voff = lane * 16;
    unsigned off = (unsigned)b * (unsigned)(RANGE_TILES * TILE_BYTES), slot = 0;
    const unsigned fulla = lbase + L_FULL * 4, ctla = lbase + (L_FREE + (lane < NC ? lane : 0)) * 4;
    int issued = 0;
#define DMA(R, S, O) __builtin_amdgcn_raw_ptr_buffer_load_lds(R, (LAS void*)((LAS unsigned char*)lds + (S)), 16, voff, off, O, 2)
#define PUB() do { asm volatile("s_waitcnt vmcnt(" STR(WIN - GL) ")" ::: "memory"); lds_poke(fulla, max(lmax, issued - (WIN - GL))); issued += GL; } while (0)
#define WONLY() do { if (GL == 1) { asm volatile("s_waitcnt vmcnt(" STR(WIN - GL) ")" ::: "memory"); issued += 1; } } while (0)
#define PART(R, S) do { PUB(); DMA(R, S, 0); WONLY(); DMA(R, S, 1024); if (GL <= 2) PUB(); DMA(R, S, 2048); WONLY(); DMA(R, S, 3072); } while (0)
#define TILE(R) do { PART(R, slot); off += TILE_BYTES; slot += TILE_BYTES; } while (0)
    int lmax = 0;
    DMA(rk, 0, 0); DMA(rk, 0, 1024); DMA(rk, 0, 2048); DMA(rk, 0, 3072);
    off += TILE_BYTES;
    DMA(rk, TILE_BYTES, 0); DMA(rk, TILE_BYTES, 1024); DMA(rk, TILE_BYTES, 2048); DMA(rk, TILE_BYTES, 3072);
    off += TILE_BYTES; slot = 2 * TILE_BYTES; issued = 8;
    if (lane < 32) lds_poke(fulla + lane * 4, 0u);
    TILE(rk); TILE(rk); TILE(rk);
    LGKM0();
    __builtin_amdgcn_s_barrier();
    TILE(rk); TILE(rk); TILE(rk);
#define RINGWAIT(t) do { \
      if (slot == RT * TILE_BYTES) slot = 0; \
      const unsigned need = (t) + 8 > RT ? (unsigned)((t) + 8 - RT) : 0u; \
      unsigned v; \
      asm volatile("ds_read_b32 %0, %1\n\ts_waitcnt lgkmcnt(0)" : "=v"(v) : "v"(ctla) : "memory"); \
      if (__any(v < need)) { \
        asm volatile("s_waitcnt vmcnt(0)" ::: "memory"); \
        lmax = issued; lds_poke(fulla, issued); \
        unsigned spins = 0; \
        do { __builtin_amdgcn_s_sleep(1); asm volatile("ds_read_b32 %0, %1\n\ts_waitcnt lgkmcnt(0)" : "=v"(v) : "v"(ctla) : "memory"); } while (__any(v < need) && ++spins < SPIN_CAP); \
      } } while (0)
    for (int t = 8; t < RANGE_TILES; t += 8) {
      RINGWAIT(t);
      TILE(rk); TILE(rk); TILE(rk); TILE(rk); TILE(rk); TILE(rk); TILE(rk); TILE(rk);
    }
    off -= RANGE_TILES * TILE_BYTES;
    for (int t = RANGE_TILES; t < 2 * RANGE_TILES; t += 8) {
      RINGWAIT(t);
      TILE(rv); TILE(rv); TILE(rv); TILE(rv); TILE(rv); TILE(rv); TILE(rv); TILE(rv);
    }
#define TAILPUB(n) if (WIN > (n)) { asm volatile("s_waitcnt vmcnt(" #n ")" ::: "memory"); lds_poke(fulla, RANGE_TILES * 8 - (n)); }
    TAILPUB(36) TAILPUB(32) TAILPUB(28) TAILPUB(24) TAILPUB(20) TAILPUB(16) TAILPUB(12) TAILPUB(8) TAILPUB(4) TAILPUB(0)
    LGKM0();
    return;
  }

  const int g = b >> 5, j = b & 31;
  const unsigned r8 = 8 * (lane & 3);
  const auto rws = RSRC(ws, WS_END);

  if (wave >= 3) {
    const int sw = wave - 3, c = lane & 31, t = lane & 15;
    constexpr float C2 = 0.0625f * 1.4426950408889634f;
    __builtin_amdgcn_s_barrier();
    u32x4 q0, q1;
    float m = -INFINITY;
    float x_[8];
    unsigned full = 0, tag;
    float* scl = reinterpret_cast<float*>(&lds[L_SC + sw * 16 + (t >> 2) * 4 + (lane >> 5) * 2 + ((t >> 1) & 1)]);
#define WAITFULL(n) do { for (unsigned spins = 0; full < (unsigned)(n) && spins < SPIN_CAP; ++spins) { full = lds_peek(lbase + L_FULL * 4); if (full >= (unsigned)(n)) break; __builtin_amdgcn_s_sleep(1); } } while (0)
#define RD4(dst, tile) do { const u32x4* src_ = reinterpret_cast<const u32x4*>(&lds[((tile) % RT) * 1024 + lane * 4]); \
      CBAR(); dst[0] = src_[0]; dst[1] = src_[64]; dst[2] = src_[128]; dst[3] = src_[192]; } while (0)
#define PIN4(a) asm volatile("" : "+v"(a[0]), "+v"(a[1]), "+v"(a[2]), "+v"(a[3]) :: "memory")
#define RELEASE(n) do { LGKM0(); lds_poke(lbase + (L_FREE + sw) * 4, (n)); } while (0)
#define QK1(KK, i) { float a_ = 0.f, b_ = 0.f; \
      _Pragma("unroll") for (int k = 0; k < 4; ++k) { a_ = dot2((KK)[k], q0[k], a_); b_ = dot2((KK)[k], q1[k], b_); } \
      x_[2 * (i)] = a_; x_[2 * (i) + 1] = b_; }
#define TR_STEP(HALF, CTRL, BIT) _Pragma("unroll") for (int h = 0; h < (HALF); ++h) { \
      const float lo_ = x_[h], hi_ = x_[h + (HALF)]; const float t1_ = lo_ + dpp<CTRL>(lo_), t2_ = hi_ + dpp<CTRL>(hi_); \
      x_[h] = (t & (BIT)) ? t2_ : t1_; }
#define SCORES(KB, n) { QK1(KB[0], 0) QK1(KB[1], 1) QK1(KB[2], 2) QK1(KB[3], 3) \
      TR_STEP(4, XOR8, 8) TR_STEP(2, XOR7, 4) TR_STEP(1, XOR2, 2) x_[0] += dpp<XOR1>(x_[0]); \
      const float sc = sw16_add(x_[0]) * C2; \
      m = fmaxf(m, sc); \
      if ((lane & 17) == 0) scl[(n) * NC * 16] = sc; }
#define ALLFLAGS(base) do { for (unsigned spins = 0; spins < SPIN_CAP; ++spins) { unsigned fl; \
      asm volatile("ds_read_b32 %0, %1\n\ts_waitcnt lgkmcnt(0)" : "=v"(fl) : "v"(lbase + ((base) + (lane & (NC - 1))) * 4) : "memory"); \
      if (__all(fl != 0u)) break; __builtin_amdgcn_s_sleep(1); } } while (0)
    {
      {
        u32x4 Kb[HD][4];
#pragma unroll
        for (int d = 0; d < HD; ++d) {
          const int tile = d * NC + sw;
          WAITFULL(tile * 4 + 4);
          RD4(Kb[d], tile);
          RELEASE(tile + NC);
          PIN4(Kb[d]);
        }
        lds_wait(lbase + L_QRDY * 4, 1u);
        q0 = *reinterpret_cast<const u32x4*>(&lds[L_Q + 4 * c]); q1 = *reinterpret_cast<const u32x4*>(&lds[L_Q + 128 + 4 * c]);
        tag = lds_peek(lbase + L_TAG * 4);
#pragma unroll
        for (int d = 0; d < HD; ++d) { SCORES(Kb[d], d) SB(); }
      }
      for (int n = HD; n < RANGE_TILES / NC; ++n) {
        const int tile = n * NC + sw;
        u32x4 Kb[4];
        WAITFULL(tile * 4 + 4);
        RD4(Kb, tile);
        RELEASE(tile + NC > RANGE_TILES ? RANGE_TILES : tile + NC);
        PIN4(Kb);
        SCORES(Kb, n)
        SB();
      }
      m = fmaxf(m, dpp<ROR4>(m)); m = fmaxf(m, dpp<XOR8>(m)); m = sw32_max(m);
      {
        float* mx = reinterpret_cast<float*>(&lds[L_MX]);
        if (lane == 0 || lane == 2) mx[sw * 2 + (lane >> 1)] = m;
        LGKM0();
        lds_poke(lbase + (L_MF + sw) * 4, 1u);
        ALLFLAGS(L_MF);
        float M0 = mx[0], M1 = mx[1];
#pragma unroll
        for (int w = 1; w < NC; ++w) { M0 = fmaxf(M0, mx[2 * w]); M1 = fmaxf(M1, mx[2 * w + 1]); }
        f32x4* ps = reinterpret_cast<f32x4*>(&lds[L_SC + sw * 1024 + lane * 4]);
        float l0 = 0.f, l1 = 0.f;
#pragma unroll
        for (int i = 0; i < 4; ++i) {
          f32x4 sv = ps[i * 64];
          sv[0] = __builtin_amdgcn_exp2f(sv[0] - M0); sv[1] = __builtin_amdgcn_exp2f(sv[1] - M1);
          sv[2] = __builtin_amdgcn_exp2f(sv[2] - M0); sv[3] = __builtin_amdgcn_exp2f(sv[3] - M1);
          l0 += sv[0] + sv[2]; l1 += sv[1] + sv[3];
          ps[i * 64] = sv;
        }
        l0 = red_all(l0); l1 = red_all(l1);
        if (lane == 0) *reinterpret_cast<f32x4*>(&lds[L_LS + sw * 4]) = f32x4{l0, l1, M0, M1};
        LGKM0();
        lds_poke(lbase + (L_PF + sw) * 4, 1u);
        ALLFLAGS(L_PF);
      }
      f32x2 acc0[4], acc1[4];
#pragma unroll
      for (int k = 0; k < 4; ++k) { acc0[k] = f32x2{0.f, 0.f}; acc1[k] = f32x2{0.f, 0.f}; }
      const unsigned* vsrc = &lds[(lane >> 3) * 128 + sw * 32 + (lane & 7) * 4];
      const float* psrc = reinterpret_cast<const float*>(&lds[L_SC + (lane >> 3) * 2]);
#define PVT(VV, PP) { _Pragma("unroll") for (int k = 0; k < 4; ++k) { const f32x2 vf = {bflo((VV)[k]), bfhi((VV)[k])}; \
        acc0[k] = __builtin_elementwise_fma(f32x2{(PP)[0], (PP)[0]}, vf, acc0[k]); acc1[k] = __builtin_elementwise_fma(f32x2{(PP)[1], (PP)[1]}, vf, acc1[k]); } }
#define PINACC() _Pragma("unroll") for (int k = 0; k < 4; ++k) { asm volatile("" : "+v"(acc0[k])); asm volatile("" : "+v"(acc1[k])); }
      for (int n = 0; n < RANGE_TILES - 4; n += 4) {
        u32x4 Vb[4]; f32x2 pp[4];
#pragma unroll
        for (int i = 0; i < 4; ++i) pp[i] = *reinterpret_cast<const f32x2*>(psrc + (n + i) * 16);
        WAITFULL((RANGE_TILES + n + 4) * 4);
        CBAR();
#pragma unroll
        for (int i = 0; i < 4; ++i) Vb[i] = *reinterpret_cast<const u32x4*>(vsrc + ((n + i) % RT) * 1024);
        RELEASE(RANGE_TILES + n + 4);
        PIN4(Vb);
        PVT(Vb[0], pp[0]) PVT(Vb[1], pp[1]) PVT(Vb[2], pp[2]) PVT(Vb[3], pp[3])
        PINACC(); SB();
      }
      lds_wait(lbase + L_WRDY * 4, 1u);
      const float wsc = reinterpret_cast<const float*>(&lds[L_WS])[lane >> 5];
#pragma unroll
      for (int i = 0; i < 4; ++i) {
        const int n = RANGE_TILES - 4 + i;
        const f32x2 pp = *reinterpret_cast<const f32x2*>(psrc + n * 16);
        WAITFULL((RANGE_TILES + n + 1) * 4);
        CBAR();
        const u32x4 Vb = *reinterpret_cast<const u32x4*>(vsrc + (n % RT) * 1024);
        PVT(Vb, pp)
        PINACC(); SB();
      }
      {
        float u[8];
#pragma unroll
        for (int k = 0; k < 4; ++k) { u[2 * k] = tr32(acc0[k][0], acc1[k][0]); u[2 * k + 1] = tr32(acc0[k][1], acc1[k][1]); }
        f32x4 r;
#pragma unroll
        for (int k = 0; k < 4; ++k) { const float v_ = tr16(u[k], u[k + 4]); r[k] = (v_ + dpp<XOR8>(v_)) * wsc; }
        const int jc = (lane >> 5) * 16 + (2 * sw + ((lane >> 2) & 1)) * 2 + ((lane >> 4) & 1);
        const u32x4 rec = rec_seal(r, tag, r8);
        if ((lane & 8) == 0) ST128(rec, rws, WS_P1 + ((g * NJ + jc) * NJ + j) * 64 + (lane & 3) * 16);
      }
      if (b < NRED && sw < 4) {
        u32x4 v[4], w_[4]; unsigned spins = 0u;
        const unsigned ybase = WS_Y + (b * NWG + sw * 64) * 64 + lane * 16;
        lds_wait(lbase + L_H1 * 4, 1u);
#define YLOAD(dst) do { CBAR(); _Pragma("unroll") for (int i = 0; i < 4; ++i) dst[i] = LD128(rws, ybase + i * 1024, 0, 16); CBAR(); } while (0)
#define YOK(src) __all((((rec_fold(src[0], r8) ^ tag) | (rec_fold(src[1], r8) ^ tag)) | ((rec_fold(src[2], r8) ^ tag) | (rec_fold(src[3], r8) ^ tag))) == 0u)
        YLOAD(v);
        __builtin_amdgcn_s_sleep(PIPE);
        for (;;) {
          YLOAD(w_);
          if (YOK(v)) break;
          YLOAD(v);
          if (YOK(w_) || ++spins > SPIN_CAP) {
#pragma unroll
            for (int i = 0; i < 4; ++i) v[i] = w_[i];
            break;
          }
        }
        float s[4];
#pragma unroll
        for (int k = 0; k < 4; ++k) s[k] = (rec_val(v[0][k]) + rec_val(v[1][k])) + (rec_val(v[2][k]) + rec_val(v[3][k]));
        const float u = red4(s[0], s[1], s[2], s[3]);
        float* fin = reinterpret_cast<float*>(&lds[L_FINV]);
        if ((lane & 12) == 0) fin[sw * 16 + (lane & 3) * 4 + (lane >> 4)] = u;
        unsigned old = 0;
        CBAR();
        if (lane == 0) old = __hip_atomic_fetch_add((LAS unsigned*)&lds[L_FIN], 1u, RLX_WG);
        old = __builtin_amdgcn_readfirstlane(old);
        CBAR();
        if (old == 3u) {
          if (lane < 16) out[b * 16 + lane] = (unsigned short)f2bf((fin[lane] + fin[16 + lane]) + (fin[32 + lane] + fin[48 + lane]));
          if (b == 0 && lane == 0) __builtin_amdgcn_raw_buffer_store_b64(u32x2{lds[L_TAG + 1], lds[L_TAG + 2]}, rws, WS_EPOCH, 0, 16);
        }
      }
    }
    return;
  }

  const int cw = wave - 1;
  __builtin_amdgcn_s_barrier();
  const unsigned tc0 = now();
  const int rr = (lane & 1) | ((lane >> 4) << 1);
  unsigned tag;
  u32x2 wov[16];
#define WO_LOAD() do { const auto ro = RSRC(wo, HQ * DH * DM * 2); const int d0 = g * 512 + (j >> 1) * 32 + (j & 1) * 4; \
    _Pragma("unroll") for (int r = 0; r < 16; ++r) wov[r] = __builtin_amdgcn_raw_buffer_load_b64(ro, (d0 + (r >> 2) * 8 + (r & 3)) * (DM * 2) + cw * 448 + lane * 8, 0, 2); } while (0)
  if (cw == 0) {
    const int qcg = b >> 2, qrg = b & 3, row0 = qrg * 112 + rr * 14;
    const auto rx = RSRC(x, DM * 2);
    const auto rq = RSRC(wq, DM * HQ * DH * 2);
    u32x4 w[14];
#pragma unroll
    for (int i = 0; i < 14; ++i) w[i] = LD128(rq, row0 * (HQ * DH * 2) + qcg * 128 + (lane & 14) * 8, i * (HQ * DH * 2), 2);
    const u32x4 xa = LD128(rx, row0 * 2, 0, 0);
    const u32x3 xb = __builtin_amdgcn_raw_buffer_load_b96(rx, row0 * 2 + 16, 0, 0);
    const u32x2 ep = __builtin_amdgcn_raw_buffer_load_b64(rws, WS_EPOCH, 0, 16);
    SB();
    WO_LOAD();
    SB();
    const unsigned xw[7] = {xa[0], xa[1], xa[2], xa[3], xb[0], xb[1], xb[2]};
    f32x2 a[4];
#pragma unroll
    for (int k = 0; k < 4; ++k) a[k] = f32x2{0.f, 0.f};
#pragma unroll
    for (int i = 0; i < 14; ++i) {
      const float xv = (i & 1) ? bfhi(xw[i >> 1]) : bflo(xw[i >> 1]);
#pragma unroll
      for (int k = 0; k < 4; ++k) a[k] = __builtin_elementwise_fma(f32x2{xv, xv}, f32x2{bflo(w[i][k]), bfhi(w[i][k])}, a[k]);
    }
    const unsigned long long tg = (((unsigned long long)ep[1] << 32) | ep[0]) + 1ull;
    tag = 2u * (unsigned)tg + 1u;
    {
      const u32x4 rec = rec_seal(proj_reduce(a, lane), tag, r8);
      if (lane < 16) ST128(rec, rws, WS_QG + (qrg * 4096 + qcg * 64) * 4 + lane * 16);
    }
    u32x4 gq[8]; unsigned go[8], spins = 0;
#pragma unroll
    for (int i = 0; i < 8; ++i) { go[i] = WS_QG + ((i >> 1) * 4096 + g * 512 + (i & 1) * 256) * 4 + lane * 16; gq[i] = u32x4{0u, 0u, 0u, 0u}; }
    while (now() - tc0 < QPOLL_T) __builtin_amdgcn_s_sleep(2);
    for (;;) {
      CBAR();
      u32x4 t_[8];
#pragma unroll
      for (int i = 0; i < 8; ++i) t_[i] = LD128(rws, go[i], 0, 16);
      bool all = true;
#pragma unroll
      for (int i = 0; i < 8; ++i) {
        if (rec_fold(t_[i], r8) == tag) { gq[i] = t_[i]; go[i] = OOB; }
        all &= go[i] == OOB;
      }
      if (__all(all) || ++spins > SPIN_CAP) break;
      __builtin_amdgcn_s_sleep(8);
    }
#pragma unroll
    for (int hf = 0; hf < 2; ++hf) {
      float qs[4];
#pragma unroll
      for (int k = 0; k < 4; ++k) qs[k] = (rec_val(gq[hf][k]) + rec_val(gq[2 + hf][k])) + (rec_val(gq[4 + hf][k]) + rec_val(gq[6 + hf][k]));
      *reinterpret_cast<u32x2*>(&lds[L_Q + hf * 128 + 2 * lane]) = u32x2{f2bf(qs[0]) | (f2bf(qs[1]) << 16), f2bf(qs[2]) | (f2bf(qs[3]) << 16)};
    }
    if (lane == 0) { lds[L_TAG] = tag; lds[L_TAG + 1] = (unsigned)tg; lds[L_TAG + 2] = (unsigned)(tg >> 32); }
    LGKM0();
    lds_poke(lbase + L_QRDY * 4, 1u);
  } else {
    const bool mlp = b < 224;
    const auto rx = RSRC(x, DM * 2);
    const auto rg = RSRC(gw, DM * 2);
    const auto rb2 = RSRC(b2, DM * 2);
    const auto r1 = RSRC(w1, mlp ? DM * FF * 2 : 0);
    const auto r2 = RSRC(w2, mlp ? FF * DM * 2 : 0);
    const auto rb = RSRC(b1, FF * 2);
    const int row0 = (b & 7) * 56 + rr * 7, hc0 = (mlp ? b : 0) * 8;
    u32x4 w1v[7], wb[8]; unsigned short xr[7];
#pragma unroll
    for (int i = 0; i < 7; ++i) { w1v[i] = LD128(r1, (row0 + i) * (FF * 2) + (b >> 3) * 128 + (lane & 14) * 8, 0, 2); xr[i] = x[row0 + i]; }
    const u32x4 xv = LD128(rx, lane * 16, 0, 0), gv = LD128(rg, lane * 16, 0, 0), b2v = LD128(rb2, lane * 16, 0, 0);
    const u32x4 bv = LD128(rb, hc0 * 2, 0, 0);
#pragma unroll
    for (int i = 0; i < 8; ++i) wb[i] = LD128(r2, (hc0 + i) * (DM * 2) + lane * 16, 0, 2);
    WO_LOAD();
    SB();
    lds_wait(lbase + L_QRDY * 4, 1u);
    tag = lds_peek(lbase + L_TAG * 4);
    {
      f32x2 hv[4];
#pragma unroll
      for (int k = 0; k < 4; ++k) hv[k] = f32x2{0.f, 0.f};
#pragma unroll
      for (int i = 0; i < 7; ++i) {
        const float xf = bflo(xr[i]);
#pragma unroll
        for (int k = 0; k < 4; ++k) hv[k] = __builtin_elementwise_fma(f32x2{xf, xf}, f32x2{bflo(w1v[i][k]), bfhi(w1v[i][k])}, hv[k]);
      }
      const u32x4 rec = rec_seal(proj_reduce(hv, lane), tag, r8);
      if (mlp && lane < 16) ST128(rec, rws, WS_HP + ((b & 7) * FF + (b >> 3) * 64) * 4 + lane * 16);
    }
    float s = 0.f;
#pragma unroll
    for (int k = 0; k < 4; ++k) { s = fmaf(bflo(xv[k]), bflo(gv[k]), s); s = fmaf(bfhi(xv[k]), bfhi(gv[k]), s); }
    s = red_all(s);
    const float gate = 1.f / (1.f + __expf(-s));
    const float bsc = b == NWG - 1 ? 1.f - gate : 0.f;
    f32x2 y2[4];
#pragma unroll
    for (int k = 0; k < 4; ++k) y2[k] = f32x2{bsc * bflo(b2v[k]), bsc * bfhi(b2v[k])};
    if (mlp) {
      u32x4 hr; unsigned spins = 0;
      while (now() - tc0 < HPOLL_T) __builtin_amdgcn_s_sleep(16);
      for (;;) {
        CBAR();
        hr = LD128(rws, WS_HP + (((lane >> 2) & 7) * FF + (b >> 1) * 16) * 4 + (lane & 3) * 16, 0, 16);
        if (__all(rec_fold(hr, r8) == tag) || ++spins > SPIN_CAP) break;
        __builtin_amdgcn_s_sleep(16);
      }
      float hs[4];
#pragma unroll
      for (int k = 0; k < 4; ++k) { float v_ = rec_val(hr[k]); v_ += dpp<ROR4>(v_); v_ += dpp<XOR8>(v_); hs[k] = sw16_add(v_); }
#pragma unroll
      for (int k = 0; k < 8; ++k) {
        const float h = __uint_as_float(__builtin_amdgcn_readlane(__float_as_uint(hs[k & 3]), (b & 1) * 2 + (k >> 2))) + ((k & 1) ? bfhi(bv[k >> 1]) : bflo(bv[k >> 1]));
        const float u = 0.7978845608028654f * (h + 0.044715f * h * h * h);
        const float ge = 0.5f * h * (1.f + tanhf(u)) * (1.f - gate);
#pragma unroll
        for (int k2 = 0; k2 < 4; ++k2) y2[k2] = __builtin_elementwise_fma(f32x2{ge, ge}, f32x2{bflo(wb[k][k2]), bfhi(wb[k][k2])}, y2[k2]);
      }
    }
    float* yl = reinterpret_cast<float*>(&lds[L_Y2]);
    *reinterpret_cast<f32x4*>(yl + 8 * lane) = f32x4{y2[0][0], y2[0][1], y2[1][0], y2[1][1]};
    *reinterpret_cast<f32x4*>(yl + 8 * lane + 4) = f32x4{y2[2][0], y2[2][1], y2[3][0], y2[3][1]};
    if (lane == 0) yl[512] = gate;
    LGKM0();
    lds_poke(lbase + L_YRDY * 4, 1u);
  }
  f32x2 wof[16][2];
#pragma unroll
  for (int r = 0; r < 16; ++r)
#pragma unroll
    for (int k = 0; k < 2; ++k) { wof[r][k] = f32x2{bflo(wov[r][k]), bfhi(wov[r][k])}; asm volatile("" : "+v"(wof[r][k])); }
  lds_wait(lbase + L_YRDY * 4, 1u);
  const float* yl = reinterpret_cast<const float*>(&lds[L_Y2]);
  const f32x4 ya = *reinterpret_cast<const f32x4*>(yl + 224 * cw + 4 * lane);
  if (cw == 0) {
    const float gate = yl[512];
    for (unsigned spins = 0; spins < SPIN_CAP; ++spins) {
      unsigned fl;
      asm volatile("ds_read_b32 %0, %1\n\ts_waitcnt lgkmcnt(0)" : "=v"(fl) : "v"(lbase + (L_PF + (lane & (NC - 1))) * 4) : "memory");
      if (__all(fl != 0u)) break;
      __builtin_amdgcn_s_sleep(8);
    }
    const float* ls = reinterpret_cast<const float*>(&lds[L_LS]);
    const int h = lane >> 5;
    const float mo = ls[2 + h], lo_ = (ls[h] + ls[4 + h]) + (ls[8 + h] + ls[12 + h]);
    if ((lane & 31) == 0) ST128((u32x4{__float_as_uint(mo), __float_as_uint(lo_), __float_as_uint(mo) ^ rotl(__float_as_uint(lo_), 16) ^ tag, 0u}), rws, WS_ML + ((g * 2 + h) * NJ + j) * 16);
    u32x4 ml;
    const unsigned tw = now();
    while (now() - tw < MLPOLL_T) __builtin_amdgcn_s_sleep(16);
    for (unsigned spins = 0;; ++spins) {
      CBAR();
      ml = LD128(rws, WS_ML + (g * 2 * NJ + lane) * 16, 0, 16);
      if (__all((ml[0] ^ rotl(ml[1], 16) ^ ml[2]) == tag) || spins > SPIN_CAP) break;
      __builtin_amdgcn_s_sleep(32);
    }
    const float ms = __uint_as_float(ml[0]);
    float M = ms;
    M = fmaxf(M, dpp<XOR1>(M)); M = fmaxf(M, dpp<XOR2>(M)); M = fmaxf(M, dpp<ROR4>(M)); M = fmaxf(M, dpp<XOR8>(M)); M = sw16_max(M);
    float L = __builtin_amdgcn_exp2f(ms - M) * __uint_as_float(ml[1]);
    L += dpp<XOR1>(L); L += dpp<XOR2>(L); L += dpp<ROR4>(L); L += dpp<XOR8>(L); L = sw16_add(L);
    if ((lane & 31) == 0) reinterpret_cast<float*>(&lds[L_WS])[h] = gate * __builtin_amdgcn_exp2f(mo - M) / L;
    LGKM0();
    lds_poke(lbase + L_WRDY * 4, 1u);
  }
  {
    for (unsigned spins = 0; lds_peek(lbase + L_FULL * 4) < (unsigned)(RANGE_TILES * 8) && spins < SPIN_CAP; ++spins) __builtin_amdgcn_s_sleep(4);
    u32x4 pa, pb, pa2, pb2; unsigned spins = 0u;
    const unsigned pbase = WS_P1 + (g * NJ + j) * NJ * 64 + lane * 16;
#define HLOAD(A, B) do { CBAR(); A = LD128(rws, pbase, 0, 16); B = LD128(rws, pbase + 1024, 0, 16); CBAR(); } while (0)
#define HBAD(A, B) __builtin_amdgcn_ballot_w64(((rec_fold(A, r8) ^ tag) | (rec_fold(B, r8) ^ tag)) != 0u)
    unsigned long long bad;
    for (;;) {
      HLOAD(pa, pb);
      bad = HBAD(pa, pb);
      if (__builtin_popcountll(bad) <= LAZYN || ++spins > SPIN_CAP) break;
      __builtin_amdgcn_s_sleep(LAZY);
    }
    if (bad) {
      HLOAD(pa, pb);
      __builtin_amdgcn_s_sleep(PIPE);
      for (;;) {
        HLOAD(pa2, pb2);
        if (!HBAD(pa, pb)) break;
        HLOAD(pa, pb);
        if (!HBAD(pa2, pb2) || ++spins > SPIN_CAP) { pa = pa2; pb = pb2; break; }
      }
    }
    const float u = red4(rec_val(pa[0]) + rec_val(pb[0]), rec_val(pa[1]) + rec_val(pb[1]), rec_val(pa[2]) + rec_val(pb[2]), rec_val(pa[3]) + rec_val(pb[3]));
    f32x2 y[2], z[2];
#define OROW(r) __uint_as_float(__builtin_amdgcn_readlane(__float_as_uint(u), 16 * ((r) & 3) + ((r) >> 2)))
    { const float o0 = OROW(0), o1 = OROW(1);
      y[0] = __builtin_elementwise_fma(f32x2{o0, o0}, wof[0][0], f32x2{ya[0], ya[1]}); y[1] = __builtin_elementwise_fma(f32x2{o0, o0}, wof[0][1], f32x2{ya[2], ya[3]});
      z[0] = o1 * wof[1][0]; z[1] = o1 * wof[1][1]; }
#define YR2(r) { const float o0 = OROW(r), o1 = OROW((r) + 1); \
      y[0] = __builtin_elementwise_fma(f32x2{o0, o0}, wof[r][0], y[0]); y[1] = __builtin_elementwise_fma(f32x2{o0, o0}, wof[r][1], y[1]); \
      z[0] = __builtin_elementwise_fma(f32x2{o1, o1}, wof[(r) + 1][0], z[0]); z[1] = __builtin_elementwise_fma(f32x2{o1, o1}, wof[(r) + 1][1], z[1]); }
    YR2(2) YR2(4) YR2(6) YR2(8) YR2(10) YR2(12) YR2(14)
    y[0] += z[0]; y[1] += z[1];
    const u32x4 yv = rec_seal(f32x4{y[0][0], y[0][1], y[1][0], y[1][1]}, tag, r8);
    if (lane < 56) ST128(yv, rws, WS_Y + (((cw * 14 + (lane >> 2)) * NWG + b) * 64 + (lane & 3) * 16));
    if (cw == 0) { lds_poke(lbase + L_H1 * 4, 1u); }
  }
}

extern "C" void kernel_launch(void* const* d_in, const int* in_sizes, int n_in, void* d_out, int out_size, void* d_ws, size_t ws_size, hipStream_t stream) {
  k_fused<<<NWG, NTHR, 0, stream>>>((const unsigned char*)d_in[1], (const unsigned char*)d_in[2], (const unsigned short*)d_in[0], (const unsigned short*)d_in[3],
                                    (const unsigned short*)d_in[4], (const unsigned short*)d_in[6], (const unsigned short*)d_in[7], (const unsigned short*)d_in[8],
                                    (const unsigned short*)d_in[9], (const unsigned short*)d_in[10], (unsigned short*)d_out, (unsigned char*)d_ws);
}
```
